```python
import torch

def _softmax(x, axis=-1):
    return torch.softmax(x, dim=axis)

def _gelu(x):
    return torch.nn.functional.gelu(x, approximate='tanh')

def _silu(x):
    return torch.nn.functional.silu(x)

def _sigmoid(x):
    return torch.sigmoid(x)

def _layernorm(x, axis=-1, eps=1e-05):
    m = x.mean(dim=axis, keepdim=True)
    v = x.var(dim=axis, keepdim=True, unbiased=False)
    return (x - m) / torch.sqrt(v + eps)

def _scatter_add(tgt, idx, src, axis=0):
    return tgt.clone().index_add_(axis, idx, src)

def _segment_sum(src, seg, n):
    out = torch.zeros((n,) + src.shape[1:], dtype=src.dtype)
    return out.index_add_(0, seg, src)

def _pad(x, pw):
    flat = tuple((v for pair in reversed(pw) for v in pair))
    return torch.nn.functional.pad(x, flat)
kernel_name = 'adaLN_cond__compressed_kv_cla__geglu_ffn__decode'

def setup_inputs():

    _w = lambda a: (lambda t: t.to(torch.bfloat16) if t.is_floating_point() else t)(torch.as_tensor(a))

    def _A_setup():
        B, H, Sq, Sk, D, M = (1, 32, 1, 16384, 128, 576)
        Dc = 4 * D
        return {'x': _w((lambda *s: torch.randn(*s, dtype=torch.bfloat16) * 0.05)(B, Sq, M)), 'c_kv': _w((lambda *s: torch.randn(*s, dtype=torch.bfloat16) * 1.6)(B, Sk, Dc)), 'w_q': _w((lambda *s: torch.randn(*s, dtype=torch.bfloat16) * 0.8)(M, H, D) * M ** (-0.5)), 'w_uk': _w((lambda *s: torch.randn(*s, dtype=torch.bfloat16) * 0.8)(Dc, H, D) * Dc ** (-0.5)), 'w_uv': _w((lambda *s: torch.randn(*s, dtype=torch.bfloat16) * 1.6)(Dc, H, D) * Dc ** (-0.5)), 'w_o': _w((lambda *s: torch.randn(*s, dtype=torch.bfloat16) * 1.6)(H, D, M) * (H * D) ** (-0.5))}
    _A_in = _A_setup()

    def _B_setup():
        B, S, D, F = (1, 1, 576, 4608)
        return {'x': _w((lambda *s: torch.randn(*s, dtype=torch.bfloat16) * 0.05)(B, S, D)), 'w_gate': _w((lambda *s: torch.randn(*s, dtype=torch.bfloat16) * 0.05)(D, F)), 'w_up': _w((lambda *s: torch.randn(*s, dtype=torch.bfloat16) * 0.05)(D, F)), 'w_down': _w((lambda *s: torch.randn(*s, dtype=torch.bfloat16) * 0.05)(F, D))}
    _B_in = _B_setup()
    return {'A_x': _A_in['x'], 'A_c_kv': _A_in['c_kv'], 'A_w_q': _A_in['w_q'], 'A_w_uk': _A_in['w_uk'], 'A_w_uv': _A_in['w_uv'], 'A_w_o': _A_in['w_o'], 'B_x': _B_in['x'], 'B_w_gate': _B_in['w_gate'], 'B_w_up': _B_in['w_up'], 'B_w_down': _B_in['w_down'], 'c': _w((lambda *s: torch.randn(*s, dtype=torch.bfloat16) * 0.1)(1, 576) * 8.0), 'w_mod': _w((lambda *s: torch.randn(*s, dtype=torch.bfloat16) * 0.1)(576, 6 * 576) * 576 ** (-0.5) * 8.0)}

def reference(A_x, A_c_kv, A_w_q, A_w_uk, A_w_uv, A_w_o, B_x, B_w_gate, B_w_up, B_w_down, c, w_mod):

    def _A_ref(x, c_kv, w_q, w_uk, w_uv, w_o):
        B, Sq, M = (x.shape[0], x.shape[1], x.shape[2])
        Sk = c_kv.shape[1]
        H, D = (w_q.shape[1], w_q.shape[2])
        q = torch.einsum('bqm,mhd->bhqd', x, w_q)
        k = torch.einsum('bkc,chd->bhkd', c_kv, w_uk)
        v = torch.einsum('bkc,chd->bhkd', c_kv, w_uv)
        scores = torch.einsum('bhqd,bhkd->bhqk', q, k) * D ** (-0.5)
        off = Sk - Sq
        qi = torch.arange(Sq)[:, None]
        kj = torch.arange(Sk)[None, :]
        cmask = kj <= qi + off
        scores = torch.where(cmask[None, None], scores, -1000000000.0)
        p = _softmax(scores, axis=-1)
        ctx = torch.einsum('bhqk,bhkd->bhqd', p, v)
        return torch.einsum('bhqd,hdm->bqm', ctx, w_o)

    def _B_ref(x, w_gate, w_up, w_down):
        return _gelu(x @ w_gate) * (x @ w_up) @ w_down
    x = A_x
    mod = (_silu(c) @ w_mod).reshape(c.shape[0], 6, 1, -1)
    sa, ca, ga, sb, cb, gb = (mod[:, 0].clone(), mod[:, 1].clone(), mod[:, 2].clone(), mod[:, 3].clone(), mod[:, 4].clone(), mod[:, 5].clone())
    h = _layernorm(x, axis=-1) * (1 + ca) + sa
    x = x + ga * _A_ref(h, A_c_kv, A_w_q, A_w_uk, A_w_uv, A_w_o)
    h = _layernorm(x, axis=-1) * (1 + cb) + sb
    x = x + gb * _B_ref(h, B_w_gate, B_w_up, B_w_down)
    return x
```

```cpp
#include <hip/hip_runtime.h>
#include <hip/hip_bf16.h>
typedef unsigned short u16;
__device__ __forceinline__ float bf2f(u16 b){ return __uint_as_float(((unsigned)b)<<16); }
__device__ __forceinline__ u16 f2bf(float f){ unsigned u=__float_as_uint(f); u += 0x7fffu + ((u>>16)&1u); return (u16)(u>>16); }
__device__ __forceinline__ float rbf(float f){ return bf2f(f2bf(f)); }
constexpr int M=576,H=32,D=128,DC=512,SK=16384,F=4608;

__global__ void k_mod(const u16* c,const u16* wmod,float* mod){
  __shared__ float s[M];
  for(int i=threadIdx.x;i<M;i+=blockDim.x){ float v=bf2f(c[i]); s[i]=rbf(v/(1.f+__expf(-v))); }
  __syncthreads();
  int n=blockIdx.x*blockDim.x+threadIdx.x;
  if(n<6*M){ float a=0; for(int k=0;k<M;k++) a+=s[k]*bf2f(wmod[k*6*M+n]); mod[n]=rbf(a);} }

__device__ void ln_mod(const float* xs,float* hs,const float* sh,const float* sc){
  float m=0; for(int i=0;i<M;i++) m+=xs[i]; m/=M;
  float v=0; for(int i=0;i<M;i++){ float d=xs[i]-m; v+=d*d;} v/=M;
  float r=1.f/sqrtf(v+1e-5f);
  for(int i=threadIdx.x;i<M;i+=blockDim.x) hs[i]=rbf(((xs[i]-m)*r)*(1.f+sc[i])+sh[i]);
  __syncthreads();
}
__global__ void k_q(const u16* x,const float* mod,const u16* wq,float* q){
  __shared__ float xs[M],hs[M];
  for(int i=threadIdx.x;i<M;i+=blockDim.x) xs[i]=bf2f(x[i]);
  __syncthreads();
  ln_mod(xs,hs,mod,mod+M);
  int n=blockIdx.x*blockDim.x+threadIdx.x;
  float a=0; for(int k=0;k<M;k++) a+=hs[k]*bf2f(wq[k*H*D+n]); q[n]=rbf(a);
}
__global__ void k_qabs(const float* q,const u16* wuk,float* qa){
  int i=blockIdx.x*blockDim.x+threadIdx.x; int h=i/DC,c=i%DC;
  float a=0; for(int d=0;d<D;d++) a+=q[h*D+d]*bf2f(wuk[(c*H+h)*D+d]); qa[i]=a*0.08838834764831845f;
}
__global__ void k_scores(const float* qa,const u16* ckv,float* sc){
  __shared__ float qs[H*DC/2];
  int k=blockIdx.x*64+(threadIdx.x&63); int hg=threadIdx.x>>6;
  for(int half=0;half<2;half++){
    __syncthreads();
    for(int i=threadIdx.x;i<H*DC/2;i+=256) qs[i]=qa[half*H*DC/2+i];
    __syncthreads();
    float a[4]={0,0,0,0};
    for(int c=0;c<DC;c++){ float v=bf2f(ckv[(size_t)k*DC+c]); for(int j=0;j<4;j++) a[j]+=v*qs[(hg*4+j)*DC+c]; }
    for(int j=0;j<4;j++) sc[(half*16+hg*4+j)*SK+k]=a[j];
  }
}
__global__ void k_soft(float* sc){
  __shared__ float red[256];
  float* s=sc+blockIdx.x*SK; float m=-1e30f;
  for(int i=threadIdx.x;i<SK;i+=256) m=fmaxf(m,s[i]);
  red[threadIdx.x]=m; __syncthreads();
  for(int o=128;o;o>>=1){ if(threadIdx.x<o) red[threadIdx.x]=fmaxf(red[threadIdx.x],red[threadIdx.x+o]); __syncthreads(); }
  m=red[0]; __syncthreads();
  float l=0; for(int i=threadIdx.x;i<SK;i+=256){ float e=__expf(s[i]-m); s[i]=e; l+=e; }
  red[threadIdx.x]=l; __syncthreads();
  for(int o=128;o;o>>=1){ if(threadIdx.x<o) red[threadIdx.x]+=red[threadIdx.x+o]; __syncthreads(); }
  l=1.f/red[0];
  for(int i=threadIdx.x;i<SK;i+=256) s[i]*=l;
}
__global__ void k_pv(const float* sc,const u16* ckv,float* part){
  int c=threadIdx.x; int k0=blockIdx.x*128;
  __shared__ float ps[H*128];
  for(int i=threadIdx.x;i<H*128;i+=512) ps[i]=sc[(i/128)*SK+k0+(i%128)];
  __syncthreads();
  float a[H]; for(int h=0;h<H;h++) a[h]=0;
  for(int k=0;k<128;k++){ float v=bf2f(ckv[(size_t)(k0+k)*DC+c]);
    #pragma unroll
    for(int h=0;h<H;h++) a[h]+=v*ps[h*128+k]; }
  for(int h=0;h<H;h++) part[((size_t)blockIdx.x*H+h)*DC+c]=a[h];
}
__global__ void k_ctx(const float* part,const u16* wuv,float* ctx){
  int h=blockIdx.x; __shared__ float cs[DC];
  int c=threadIdx.x; float a=0; for(int s=0;s<128;s++) a+=part[((size_t)s*H+h)*DC+c]; cs[c]=a; __syncthreads();
  if(threadIdx.x<D){ float o=0; for(int cc=0;cc<DC;cc++) o+=cs[cc]*bf2f(wuv[(cc*H+h)*D+threadIdx.x]); ctx[h*D+threadIdx.x]=rbf(o);} }
__global__ void k_o(const float* ctx,const u16* wo,const u16* x,const float* mod,float* x2){
  int m=blockIdx.x*64+(threadIdx.x&63); int g=threadIdx.x>>6; __shared__ float red[16][64];
  float a=0; for(int k=g*256;k<g*256+256;k++) a+=ctx[k]*bf2f(wo[k*M+m]); red[g][threadIdx.x&63]=a; __syncthreads();
  if(g==0){ float t=0; for(int j=0;j<16;j++) t+=red[j][threadIdx.x]; x2[m]=rbf(bf2f(x[m])+rbf(mod[2*M+m]*rbf(t))); } }
__global__ void k_gu(const float* x2,const float* mod,const u16* wg,const u16* wu,float* f){
  __shared__ float xs[M],hs[M];
  for(int i=threadIdx.x;i<M;i+=blockDim.x) xs[i]=x2[i];
  __syncthreads();
  ln_mod(xs,hs,mod+3*M,mod+4*M);
  int n=blockIdx.x*blockDim.x+threadIdx.x;
  float g=0,u=0; for(int k=0;k<M;k++){ g+=hs[k]*bf2f(wg[k*F+n]); u+=hs[k]*bf2f(wu[k*F+n]); }
  g=rbf(g); u=rbf(u);
  float t=tanhf(0.7978845608028654f*(g+0.044715f*g*g*g)); f[n]=rbf(rbf(0.5f*g*(1.f+t))*u);
}
__global__ void k_down(const float* f,const u16* wd,const float* x2,const float* mod,u16* out){
  int m=blockIdx.x*64+(threadIdx.x&63); int g=threadIdx.x>>6; __shared__ float red[16][64];
  float a=0; for(int k=g*288;k<g*288+288;k++) a+=f[k]*bf2f(wd[k*M+m]); red[g][threadIdx.x&63]=a; __syncthreads();
  if(g==0){ float t=0; for(int j=0;j<16;j++) t+=red[j][threadIdx.x]; out[m]=f2bf(x2[m]+rbf(mod[5*M+m]*rbf(t))); } }

extern "C" void kernel_launch(void* const* d_in, const int* in_sizes, int n_in, void* d_out, int out_size, void* d_ws, size_t ws_size, hipStream_t stream){
  const u16 *x=(const u16*)d_in[0],*ckv=(const u16*)d_in[1],*wq=(const u16*)d_in[2],*wuk=(const u16*)d_in[3],*wuv=(const u16*)d_in[4],*wo=(const u16*)d_in[5];
  const u16 *wg=(const u16*)d_in[7],*wu=(const u16*)d_in[8],*wd=(const u16*)d_in[9],*c=(const u16*)d_in[10],*wmod=(const u16*)d_in[11];
  float* ws=(float*)d_ws; float* mod=ws; float* q=mod+4096; float* qa=q+4096; float* ctx=qa+H*DC; float* x2=ctx+4096; float* f=x2+1024; float* sc=f+8192; float* part=sc+H*SK;
  k_mod<<<54,64,0,stream>>>(c,wmod,mod);
  k_q<<<64,64,0,stream>>>(x,mod,wq,q);
  k_qabs<<<64,256,0,stream>>>(q,wuk,qa);
  k_scores<<<256,256,0,stream>>>(qa,ckv,sc);
  k_soft<<<32,256,0,stream>>>(sc);
  k_pv<<<128,512,0,stream>>>(sc,ckv,part);
  k_ctx<<<32,512,0,stream>>>(part,wuv,ctx);
  k_o<<<9,1024,0,stream>>>(ctx,wo,x,mod,x2);
  k_gu<<<72,64,0,stream>>>(x2,mod,wg,wu,f);
  k_down<<<9,1024,0,stream>>>(f,wd,x2,mod,(u16*)d_out);
}
```

```cpp
#include <hip/hip_runtime.h>
#include <hip/hip_bf16.h>
typedef unsigned short u16;
__device__ __forceinline__ float bf2f(u16 b){ return __uint_as_float(((unsigned)b)<<16); }
__device__ __forceinline__ u16 f2bf(float f){ unsigned u=__float_as_uint(f); u += 0x7fffu + ((u>>16)&1u); return (u16)(u>>16); }
__device__ __forceinline__ float rbf(float f){ return bf2f(f2bf(f)); }
constexpr int M=576,H=32,D=128,DC=512,SK=16384,F=4608;

__global__ void k_mod(const u16* c,const u16* wmod,float* mod){
  __shared__ float s[M];
  for(int i=threadIdx.x;i<M;i+=blockDim.x){ float v=bf2f(c[i]); s[i]=rbf(v/(1.f+__expf(-v))); }
  __syncthreads();
  int n=blockIdx.x*blockDim.x+threadIdx.x;
  if(n<6*M){ float a=0; for(int k=0;k<M;k++) a+=s[k]*bf2f(wmod[k*6*M+n]); mod[n]=rbf(a);} }

__device__ void ln_mod(const float* xs,float* hs,const float* sh,const float* sc){
  float m=0; for(int i=0;i<M;i++) m+=xs[i]; m/=M;
  float v=0; for(int i=0;i<M;i++){ float d=xs[i]-m; v+=d*d;} v/=M;
  float r=1.f/sqrtf(v+1e-5f);
  for(int i=threadIdx.x;i<M;i+=blockDim.x) hs[i]=rbf(((xs[i]-m)*r)*(1.f+sc[i])+sh[i]);
  __syncthreads();
}
__global__ void k_q(const u16* x,const float* mod,const u16* wq,float* q){
  __shared__ float xs[M],hs[M];
  for(int i=threadIdx.x;i<M;i+=blockDim.x) xs[i]=bf2f(x[i]);
  __syncthreads();
  ln_mod(xs,hs,mod,mod+M);
  int n=blockIdx.x*blockDim.x+threadIdx.x;
  float a=0; for(int k=0;k<M;k++) a+=hs[k]*bf2f(wq[k*H*D+n]); q[n]=rbf(a);
}
__global__ void k_qabs(const float* q,const u16* wuk,float* qa){
  int i=blockIdx.x*blockDim.x+threadIdx.x; int h=i/DC,c=i%DC;
  float a=0; for(int d=0;d<D;d++) a+=q[h*D+d]*bf2f(wuk[(c*H+h)*D+d]); qa[i]=a*0.08838834764831845f;
}
__global__ void k_scores(const float* qa,const u16* ckv,float* sc){
  __shared__ float qs[H*DC/2];
  int k=blockIdx.x*64+(threadIdx.x&63); int hg=threadIdx.x>>6;
  for(int half=0;half<2;half++){
    __syncthreads();
    for(int i=threadIdx.x;i<H*DC/2;i+=256) qs[i]=qa[half*H*DC/2+i];
    __syncthreads();
    float a[4]={0,0,0,0};
    for(int c=0;c<DC;c++){ float v=bf2f(ckv[(size_t)k*DC+c]); for(int j=0;j<4;j++) a[j]+=v*qs[(hg*4+j)*DC+c]; }
    for(int j=0;j<4;j++) sc[(half*16+hg*4+j)*SK+k]=a[j];
  }
}
__global__ void k_soft(float* sc){
  __shared__ float red[256];
  float* s=sc+blockIdx.x*SK; float m=-1e30f;
  for(int i=threadIdx.x;i<SK;i+=256) m=fmaxf(m,s[i]);
  red[threadIdx.x]=m; __syncthreads();
  for(int o=128;o;o>>=1){ if(threadIdx.x<o) red[threadIdx.x]=fmaxf(red[threadIdx.x],red[threadIdx.x+o]); __syncthreads(); }
  m=red[0]; __syncthreads();
  float l=0; for(int i=threadIdx.x;i<SK;i+=256){ float e=__expf(s[i]-m); s[i]=e; l+=e; }
  red[threadIdx.x]=l; __syncthreads();
  for(int o=128;o;o>>=1){ if(threadIdx.x<o) red[threadIdx.x]+=red[threadIdx.x+o]; __syncthreads(); }
  l=1.f/red[0];
  for(int i=threadIdx.x;i<SK;i+=256) s[i]*=l;
}
__global__ void k_pv(const float* sc,const u16* ckv,float* part){
  int c=threadIdx.x; int k0=blockIdx.x*128;
  __shared__ float ps[H*128];
  for(int i=threadIdx.x;i<H*128;i+=512) ps[i]=sc[(i/128)*SK+k0+(i%128)];
  __syncthreads();
  float a[H]; for(int h=0;h<H;h++) a[h]=0;
  for(int k=0;k<128;k++){ float v=bf2f(ckv[(size_t)(k0+k)*DC+c]);
    #pragma unroll
    for(int h=0;h<H;h++) a[h]+=v*ps[h*128+k]; }
  for(int h=0;h<H;h++) part[((size_t)blockIdx.x*H+h)*DC+c]=a[h];
}
__global__ void k_ctx(const float* part,const u16* wuv,float* ctx){
  int h=blockIdx.x; __shared__ float cs[DC];
  int c=threadIdx.x; float a=0; for(int s=0;s<128;s++) a+=part[((size_t)s*H+h)*DC+c]; cs[c]=a; __syncthreads();
  if(threadIdx.x<D){ float o=0; for(int cc=0;cc<DC;cc++) o+=cs[cc]*bf2f(wuv[(cc*H+h)*D+threadIdx.x]); ctx[h*D+threadIdx.x]=rbf(o);} }
__global__ void k_o(const float* ctx,const u16* wo,const u16* x,const float* mod,float* x2){
  int m=blockIdx.x*64+(threadIdx.x&63); int g=threadIdx.x>>6; __shared__ float red[16][64];
  float a=0; for(int k=g*256;k<g*256+256;k++) a+=ctx[k]*bf2f(wo[k*M+m]); red[g][threadIdx.x&63]=a; __syncthreads();
  if(g==0){ float t=0; for(int j=0;j<16;j++) t+=red[j][threadIdx.x]; x2[m]=rbf(bf2f(x[m])+rbf(mod[2*M+m]*rbf(t))); } }
__global__ void k_gu(const float* x2,const float* mod,const u16* wg,const u16* wu,float* f){
  __shared__ float xs[M],hs[M];
  for(int i=threadIdx.x;i<M;i+=blockDim.x) xs[i]=x2[i];
  __syncthreads();
  ln_mod(xs,hs,mod+3*M,mod+4*M);
  int n=blockIdx.x*blockDim.x+threadIdx.x;
  float g=0,u=0; for(int k=0;k<M;k++){ g+=hs[k]*bf2f(wg[k*F+n]); u+=hs[k]*bf2f(wu[k*F+n]); }
  g=rbf(g); u=rbf(u);
  float t=tanhf(0.7978845608028654f*(g+0.044715f*g*g*g)); f[n]=rbf(rbf(0.5f*g*(1.f+t))*u);
}
__global__ void k_down(const float* f,const u16* wd,const float* x2,const float* mod,u16* out){
  int m=blockIdx.x*64+(threadIdx.x&63); int g=threadIdx.x>>6; __shared__ float red[16][64];
  float a=0; for(int k=g*288;k<g*288+288;k++) a+=f[k]*bf2f(wd[k*M+m]); red[g][threadIdx.x&63]=a; __syncthreads();
  if(g==0){ float t=0; for(int j=0;j<16;j++) t+=red[j][threadIdx.x]; out[m]=f2bf(x2[m]+rbf(mod[5*M+m]*rbf(t))); } }


typedef unsigned v4u __attribute__((ext_vector_type(4)));
#define GAS __attribute__((address_space(1)))
struct PP { const v4u* p[9]; int n[9]; int pad; unsigned* sink; };
__global__ void __launch_bounds__(512,1) k_stream(PP a){
  extern __shared__ char lds[];
  unsigned long long t0=__builtin_amdgcn_s_memrealtime();
  int w=__builtin_amdgcn_readfirstlane(threadIdx.x>>6);
  for(int m=0;m<9;m++){
    int per=a.n[m];
    int lo=(int)((long long)per*blockIdx.x/256), hi=(int)((long long)per*(blockIdx.x+1)/256);
    for(int i=lo+w;i<hi;i+=8)
      __builtin_amdgcn_global_load_lds((const GAS unsigned*)(a.p[m]+(size_t)i*64+(threadIdx.x&63)),(__attribute__((address_space(3))) unsigned*)(lds+(i&63)*1024),16,0,2);
  }
  asm volatile("s_waitcnt vmcnt(0)":::"memory");
  __syncthreads();
  unsigned long long t1=__builtin_amdgcn_s_memrealtime();
  unsigned long long d=t1-t0;
  while(__builtin_amdgcn_s_memrealtime()-t1 < d*199) __builtin_amdgcn_s_sleep(8);
}

extern "C" void kernel_launch(void* const* d_in, const int* in_sizes, int n_in, void* d_out, int out_size, void* d_ws, size_t ws_size, hipStream_t stream){
  const u16 *x=(const u16*)d_in[0],*ckv=(const u16*)d_in[1],*wq=(const u16*)d_in[2],*wuk=(const u16*)d_in[3],*wuv=(const u16*)d_in[4],*wo=(const u16*)d_in[5];
  const u16 *wg=(const u16*)d_in[7],*wu=(const u16*)d_in[8],*wd=(const u16*)d_in[9],*c=(const u16*)d_in[10],*wmod=(const u16*)d_in[11];
  float* ws=(float*)d_ws; float* mod=ws; float* q=mod+4096; float* qa=q+4096; float* ctx=qa+H*DC; float* x2=ctx+4096; float* f=x2+1024; float* sc=f+8192; float* part=sc+H*SK;

  { PP a; int idx[9]={1,2,3,4,5,7,8,9,11}; for(int i=0;i<9;i++){ a.p[i]=(const v4u*)d_in[idx[i]]; a.n[i]=in_sizes[idx[i]]*2/1024; } a.pad=0; a.sink=(unsigned*)d_ws;
    k_stream<<<256,512,65536,stream>>>(a); }
  k_mod<<<54,64,0,stream>>>(c,wmod,mod);
  k_q<<<64,64,0,stream>>>(x,mod,wq,q);
  k_qabs<<<64,256,0,stream>>>(q,wuk,qa);
  k_scores<<<256,256,0,stream>>>(qa,ckv,sc);
  k_soft<<<32,256,0,stream>>>(sc);
  k_pv<<<128,512,0,stream>>>(sc,ckv,part);
  k_ctx<<<32,512,0,stream>>>(part,wuv,ctx);
  k_o<<<9,1024,0,stream>>>(ctx,wo,x,mod,x2);
  k_gu<<<72,64,0,stream>>>(x2,mod,wg,wu,f);
  k_down<<<9,1024,0,stream>>>(f,wd,x2,mod,(u16*)d_out);
}
```

```cpp
#include <hip/hip_runtime.h>
#include <stdint.h>

typedef unsigned u32;
typedef unsigned long long u64;
typedef unsigned short u16;
typedef u32 v4u __attribute__((ext_vector_type(4)));
typedef u32 v2u __attribute__((ext_vector_type(2)));
typedef float f4 __attribute__((ext_vector_type(4)));
typedef float f2 __attribute__((ext_vector_type(2)));
typedef float f16v __attribute__((ext_vector_type(16)));
typedef short s4 __attribute__((ext_vector_type(4)));
typedef __bf16 b8 __attribute__((ext_vector_type(8)));
typedef __bf16 b2 __attribute__((ext_vector_type(2)));
#define LAS __attribute__((address_space(3)))
#define GAS __attribute__((address_space(1)))
#define DI __device__ __forceinline__

#define A_N1 6
#define A_P1 2
#define T_F 0
#define T_O 0
#define T_M 0

constexpr u32 W_EPOCH = 0;
constexpr u32 W_RDYA = 4096, W_RDYB = 12288;
constexpr u32 W_B1 = 20480;
constexpr u32 W_SM = 49152;
constexpr u32 W_B2 = W_SM;
constexpr u32 W_B3 = W_SM + 49152;
constexpr u32 W_ML = W_SM + 81920;
constexpr u32 W_B5 = W_SM + 114688;
constexpr u32 W_B6 = W_SM + 147456;
constexpr u32 W_B7 = W_SM + 156672;
constexpr u32 W_B8 = W_SM + 165888;
constexpr u32 W_B4 = 524288;
constexpr u32 W_END = W_B4 + 4194304;
constexpr u32 W_STAMP = 8u << 20;
constexpr u32 SENT = 0xFFFFFFFFu;

constexpr u32 L_FLAG = 163776;
constexpr u32 LA_Q = 131072, LA_QS = 976;
constexpr u32 LA_P = 131072, LA_W = 139264, LA_RED = 139776;
constexpr u32 LA_MX = 162304, LA_LS = 162816;
constexpr u32 LQ_XC = 147456, LQ_SC = 149760, LQ_AX = 151040, LQ_GV = 158720, LQ_ST = 161792;
enum { F_LN = 0, F_SC = 1, F_RDY = 2, F_RSTD = 3, F_ISS = 4, F_GO = 5, F_WR = 6, F_INVL = 7, F_CNT = 8, F_RDYB = 9 };

constexpr u32 SPIN_G = 1u << 15, SPIN_L = 1u << 20;

DI u64 rtime() { return __builtin_amdgcn_s_memrealtime(); }
#define STAMP(i) do {} while (0)
#define STAMP_OUT() do {} while (0)

template <class T> DI LAS T* lp(u32 a) { return (LAS T*)(uintptr_t)a; }
DI float bflo(u32 v) { return __uint_as_float(v << 16); }
DI float bfhi(u32 v) { return __uint_as_float(v & 0xFFFF0000u); }
DI u32 pk(float a, float b) { f2 v = {a, b}; return __builtin_bit_cast(u32, __builtin_convertvector(v, b2)); }
DI f4 mfma16(v4u a, v4u b, f4 c) { return __builtin_amdgcn_mfma_f32_16x16x32_bf16(__builtin_bit_cast(b8, a), __builtin_bit_cast(b8, b), c, 0, 0, 0); }
DI f16v mfma32(v4u a, v4u b, f16v c) { return __builtin_amdgcn_mfma_f32_32x32x16_bf16(__builtin_bit_cast(b8, a), __builtin_bit_cast(b8, b), c, 0, 0, 0); }
DI float dot2(u32 a, u32 b, float c) { return __builtin_amdgcn_fdot2_f32_bf16(__builtin_bit_cast(b2, a), __builtin_bit_cast(b2, b), c, false); }
DI v2u trd(u32 a) { return __builtin_bit_cast(v2u, __builtin_amdgcn_ds_read_tr16_b64_v4i16(lp<s4>(a))); }
DI v4u trd2(u32 a0, u32 a1) { v2u x = trd(a0), y = trd(a1); return v4u{x.x, x.y, y.x, y.y}; }
template <int C> DI float dppf(float v) { return __uint_as_float(__builtin_amdgcn_update_dpp(0u, __float_as_uint(v), C, 0xF, 0xF, true)); }
DI void swap16(float& a, float& b) { auto r = __builtin_amdgcn_permlane16_swap(__float_as_uint(a), __float_as_uint(b), false, false); a = __uint_as_float(r[0]); b = __uint_as_float(r[1]); }
DI void swap32(float& a, float& b) { auto r = __builtin_amdgcn_permlane32_swap(__float_as_uint(a), __float_as_uint(b), false, false); a = __uint_as_float(r[0]); b = __uint_as_float(r[1]); }
DI float fold16(float a, float b) { swap16(a, b); return a + b; }
DI float fold32(float a, float b) { swap32(a, b); return a + b; }
DI float wsum(float v) { v += dppf<0x128>(v); v += dppf<0x124>(v); v += dppf<0x122>(v); v += dppf<0x121>(v); v = fold16(v, v); return fold32(v, v); }
DI float wmax(float v) {
  v = fmaxf(v, dppf<0x128>(v)); v = fmaxf(v, dppf<0x124>(v)); v = fmaxf(v, dppf<0x122>(v)); v = fmaxf(v, dppf<0x121>(v));
  float a = v, c = v; swap16(a, c); v = fmaxf(a, c); a = v; c = v; swap32(a, c); return fmaxf(a, c);
}
DI u32 umax3(u32 a, u32 b, u32 c) { return max(max(a, b), c); }
DI u32 vmax(u32 m, v4u v) { return umax3(umax3(m, v.x, v.y), v.z, v.w); }
#define CFENCE() asm volatile("" ::: "memory")
#define VM0() asm volatile("s_waitcnt vmcnt(0)" ::: "memory")
#define BAR() asm volatile("s_waitcnt lgkmcnt(0)\n\ts_barrier" ::: "memory")
typedef __amdgpu_buffer_rsrc_t rsrc_t;
DI v4u ld128(rsrc_t r, u32 o) { return __builtin_amdgcn_raw_buffer_load_b128(r, (int)o, 0, 16); }
DI v2u ld64(rsrc_t r, u32 o) { return __builtin_amdgcn_raw_buffer_load_b64(r, (int)o, 0, 16); }
DI u32 ld32(rsrc_t r, u32 o) { return __builtin_amdgcn_raw_buffer_load_b32(r, (int)o, 0, 16); }
DI void st128(rsrc_t r, u32 o, v4u v) { __builtin_amdgcn_raw_buffer_store_b128(v, r, (int)o, 0, 16); }
DI void st64(rsrc_t r, u32 o, v2u v) { __builtin_amdgcn_raw_buffer_store_b64(v, r, (int)o, 0, 16); }
DI void st32(rsrc_t r, u32 o, u32 v) { __builtin_amdgcn_raw_buffer_store_b32(v, r, (int)o, 0, 16); }

DI void flag_set(int f, u32 v) { CFENCE(); *(volatile LAS u32*)lp<u32>(L_FLAG + 4 * f) = v; CFENCE(); }
DI u32 flag_get(int f) { return *(volatile LAS u32*)lp<u32>(L_FLAG + 4 * f); }
DI void flag_wait(int f, u32 v) { for (u32 s = 0; s < SPIN_L; s++) { if (__builtin_amdgcn_readfirstlane(flag_get(f)) >= v) break; } CFENCE(); }
DI void flag_wait_sleep(int f, u32 v) { for (u32 s = 0; s < SPIN_L; s++) { if (__builtin_amdgcn_readfirstlane(flag_get(f)) >= v) break; __builtin_amdgcn_s_sleep(1); } CFENCE(); }

DI void dma1(u32 lds, u32 voff, const void* base) {
  asm volatile("s_mov_b32 m0, %0\n\ts_nop 0\n\tglobal_load_lds_dwordx4 %1, %2 nt" ::"s"(lds), "v"(voff), "s"(base) : "memory");
}
DI void dma_run(const void* base, u32 rowoff, u32 coloff, u32 step, int i0, int n, u32 mask, u32 lds, int n1, int p1, int p2) {
#pragma nounroll
  for (int i = i0; i < n; i++) {
    const u32 xr = (((i & 3) << 6) | (((i >> 2) & 3) << 4)) & mask;
    dma1(lds + (i - i0) * 1024, rowoff + (coloff ^ xr) + i * step, base);
    const int p = i < n1 ? p1 : p2;
    if (p == 2) asm volatile("s_waitcnt vmcnt(1)" ::: "memory");
    else if (p == 4) asm volatile("s_waitcnt vmcnt(3)" ::: "memory");
    else if (p == 8) asm volatile("s_waitcnt vmcnt(7)" ::: "memory");
  }
}
template <int NU> DI void unit_frags(u32 a, int lane, v4u (&f)[NU * 4]) {
  const u32 g = lane >> 4, q = (lane & 15) >> 2, pp = lane & 3;
  const u32 sw = (((q >> 1) & 1) << 1) | ((g & 1) << 2);
  const u32 base = a + (8 * g + q) * 128 + 8 * (pp & 1);
#pragma unroll
  for (int t = 0; t < 4; t++) {
    const u32 ad = base + (((2 * t + (pp >> 1)) ^ sw) << 4);
#pragma unroll
    for (int u = 0; u < NU; u++) f[u * 4 + t] = trd2(ad + u * 4096, ad + u * 4096 + 512);
  }
}
DI float sel4(int g, float a, float b, float c, float d) { return g == 0 ? a : g == 1 ? b : g == 2 ? c : d; }
DI float silu(float v) { return v * __builtin_amdgcn_rcpf(1.f + __builtin_amdgcn_exp2f(-1.4426950408889634f * v)); }
DI void silu96(const u16* c, int kr, int lane) {
  if (lane < 48) {
    const u32 v = ((const GAS u32*)c)[48 * kr + lane];
    *lp<u32>(LQ_SC + (48 * kr + lane) * 4) = pk(silu(bflo(v)), silu(bfhi(v)));
  }
}
DI void poll_ready(rsrc_t rw, u32 off, int lane, v2u tag) {
  for (u32 s = 0; s < SPIN_G; s++) {
    CFENCE();
    const v2u a = ld64(rw, off + lane * 64), c = ld64(rw, off + 4096 + lane * 64);
    const bool ok = (a.x == tag.x) & (a.y == tag.y) & (c.x == tag.x) & (c.y == tag.y);
    if (__all(ok)) break;
    __builtin_amdgcn_s_sleep(2);
  }
  CFENCE();
}

__global__ void __launch_bounds__(768, 3)
fused_decode(const u16* __restrict__ x, const u16* __restrict__ ckv, const u16* __restrict__ wq, const u16* __restrict__ wuk,
             const u16* __restrict__ wuv, const u16* __restrict__ wo, const u16* __restrict__ wg, const u16* __restrict__ wu,
             const u16* __restrict__ wd, const u16* __restrict__ cvec, const u16* __restrict__ wmod, u16* __restrict__ out, char* ws) {
  __shared__ __attribute__((aligned(16))) char smem[163840];
  const int tid = threadIdx.x, lane = tid & 63;
  const int w = __builtin_amdgcn_readfirstlane(tid >> 6);
  const int b = blockIdx.x, xg = b & 7, j = b >> 3;
  const bool isQ = j < 16;
  const u64 t0 = rtime();
  asm volatile("" ::"v"((LAS char*)smem));
  *lp<u32>(L_FLAG + (tid & 15) * 4) = 0;
  asm volatile("s_waitcnt lgkmcnt(0)\n\ts_barrier" ::"s"(x), "s"(ckv), "s"(wq), "s"(wuk), "s"(wuv), "s"(wo), "s"(wg), "s"(wu), "s"(wd), "s"(cvec), "s"(wmod), "s"(out), "s"(ws) : "memory");
  const rsrc_t rw = __builtin_amdgcn_make_buffer_rsrc(ws, 0, (int)W_END, 0x00020000);
  const v2u ep = ld64(rw, W_EPOCH);

  const int g4 = lane >> 4, c15 = lane & 15;
  const u32 unit_row = lane >> 3, unit_col = ((lane & 7) ^ (((lane >> 4) & 1) << 1)) << 4;
  const int s1_cb = b % 18, s1_kr = b / 18;
  const int s2_cb = j >> 1, s2_kr = 3 * (j & 1) + (w - 1);
  const int hh = 4 * xg + ((j >> 2) & 3), cq = j & 3;
  const int sidx = (j - 16) * 8 + xg;
  {
    const int kind = !isQ ? 0 : w == 0 ? 1 : w < 4 ? 2 : 3;
    const char* p = (const char*)(kind == 0 ? ckv : kind == 1 ? wmod : kind == 2 ? wq : wuk);
    const u32 rs = kind == 0 ? 1024 : kind == 1 ? 6912 : 8192;
    const u32 row0 = kind == 0 ? 128 * sidx + 32 * w : kind == 1 ? 96 * s1_kr : kind == 2 ? 96 * s2_kr : 128 * cq + 32 * (w - 4);
    const u32 col0 = kind == 0 ? 0 : kind == 1 ? s1_cb * 128 : kind == 2 ? (8 * xg + s2_cb) * 128 : hh * 256;
    const u32 sh = kind == 0 ? 6 : kind == 3 ? 4 : 3;
    const u32 m1 = kind == 0 ? 0 : kind == 3 ? 0x30 : 0x20, s1 = kind == 3 ? 0 : 1;
    const u32 mask = kind == 0 ? 0xF0 : kind == 3 ? 0xC0 : 0x40;
    const u32 lds = w * (kind == 0 ? 32768 : 12288);
    const int n = kind == 0 ? (w < 4 ? 32 : 0) : kind == 1 ? (b < 108 ? 12 : 0) : kind == 2 ? 12 : (w < 8 ? 8 : 0);
    const int n1 = kind == 0 ? A_N1 : 0, p2 = kind == 0 ? 8 : kind == 1 ? 0 : 4;
    const u32 rowoff = (lane >> sh) * rs, coloff = ((lane << 4) & ((16u << sh) - 1)) ^ ((lane << s1) & m1);
    dma_run(p + (row0 * rs + col0), rowoff, coloff, rs << (6 - sh), 0, n, mask, lds, n1, A_P1, p2);
  }
  STAMP(1);
  v2u tag;
  {
    const u64 e = (((u64)ep.y << 32) | ep.x) + 1;
    tag = v2u{(u32)e, (u32)(e >> 32)};
  }

  if (isQ) {
    if (w == 0) {
      if (b < 108) {
        VM0();
        STAMP(2);
        v4u f[12];
        unit_frags<3>(0, lane, f);
        flag_wait(F_SC, 1);
        f4 acc[4] = {};
#pragma unroll
        for (int s = 0; s < 3; s++) {
          const v4u a = *lp<v4u>(LQ_SC + (96 * s1_kr + 32 * s + 8 * g4) * 2);
#pragma unroll
          for (int t = 0; t < 4; t++) acc[t] = mfma16(a, f[s * 4 + t], acc[t]);
        }
        float v = sel4(g4, acc[0].x, acc[1].x, acc[2].x, acc[3].x);
        const int isc = s1_cb >= 9, cb = s1_cb - 9 * isc;
        if (isc) {
          flag_wait(F_LN, 1);
          const float ln = *lp<float>(LQ_XC + (64 * cb + lane) * 4) * __uint_as_float(flag_get(F_RSTD));
          v = ln * (v + (s1_kr == 0 ? 1.f : 0.f));
        }
        const u32 pr = pk(v, dppf<0xB1>(v));
        if (!(lane & 1)) st64(rw, W_B1 + (6 * isc + s1_kr) * 2304 + (32 * cb + (lane >> 1)) * 8, v2u{pr, tag.x});
        STAMP(3);
      }
    } else if (w < 4) {
      VM0();
      STAMP(2);
      v4u f[12];
      unit_frags<3>(w * 12288, lane, f);
      const u32 row = 3 * (c15 >> 2) + min(c15 & 3, 2);
      const u32 ao = W_B1 + row * 2304 + (96 * s2_kr + 8 * g4) * 4;
      v4u a[6];
      for (u32 s = 0; s < SPIN_G; s++) {
        CFENCE();
#pragma unroll
        for (int i = 0; i < 6; i++) a[i] = ld128(rw, ao + (i >> 1) * 128 + (i & 1) * 16);
        bool ok = true;
#pragma unroll
        for (int i = 0; i < 6; i++) ok &= (a[i].y == tag.x) & (a[i].w == tag.x);
        if (__all(ok)) break;
      }
      STAMP(3);
      f4 acc[4] = {};
#pragma unroll
      for (int s = 0; s < 3; s++) {
        const v4u af = v4u{a[2 * s].x, a[2 * s].z, a[2 * s + 1].x, a[2 * s + 1].z};
#pragma unroll
        for (int t = 0; t < 4; t++) acc[t] = mfma16(af, f[s * 4 + t], acc[t]);
      }
      float t4[4];
#pragma unroll
      for (int t = 0; t < 4; t++) t4[t] = acc[t].x + acc[t].y + acc[t].z;
      const float v = fold16(fold32(t4[0], t4[2]), fold32(t4[1], t4[3]));
      const u32 pr = pk(v, dppf<0xB1>(v));
      flag_wait(F_RDY, 1);
      if (!(lane & 1)) st32(rw, W_B2 + ((4 * xg + (s2_cb >> 1)) * 6 + s2_kr) * 256 + (s2_cb & 1) * 128 + lane * 2, pr);
      STAMP(4);
    } else if (w < 8) {
      VM0();
      STAMP(2);
      v4u f[8];
#pragma unroll
      for (int t = 0; t < 2; t++)
#pragma unroll
        for (int s = 0; s < 4; s++) f[t * 4 + s] = *lp<v4u>(w * 12288 + (16 * t + c15) * 256 + (((4 * s + g4) ^ c15) << 4));
      flag_wait_sleep(F_RDY, 1);
      const u32 p = 3 * ((c15 >> 2) & 1) + min(c15 & 3, 2);
      const u32 ao = W_B2 + (hh * 6 + p) * 256 + g4 * 16;
      v4u a[4];
      for (u32 s = 0; s < SPIN_G; s++) {
        CFENCE();
#pragma unroll
        for (int i = 0; i < 4; i++) a[i] = ld128(rw, ao + i * 64);
        u32 m = 0;
#pragma unroll
        for (int i = 0; i < 4; i++) m = vmax(m, a[i]);
        if (__all(m != SENT)) break;
      }
      STAMP(3);
      f4 acc[2] = {};
#pragma unroll
      for (int s = 0; s < 4; s++)
#pragma unroll
        for (int t = 0; t < 2; t++) acc[t] = mfma16(a[s], f[t * 4 + s], acc[t]);
      const float v = fold16(acc[0].x + acc[0].y + acc[0].z, acc[1].x + acc[1].y + acc[1].z) * (0.08838834764831845f * 1.4426950408889634f);
      const u32 pr = pk(v, dppf<0xB1>(v));
      if (!(lane & 33)) st32(rw, W_B3 + hh * 1024 + (128 * cq + 32 * (w - 4) + lane) * 2, pr);
      STAMP(4);
    } else if (w == 8) {
      float xv[9], s = 0.f;
#pragma unroll
      for (int i = 0; i < 9; i++) xv[i] = bflo(((const GAS u16*)x)[lane + 64 * i]);
#pragma unroll
      for (int i = 0; i < 9; i++) s += xv[i];
      const float mean = wsum(s) * (1.f / 576.f);
      float q = 0.f;
#pragma unroll
      for (int i = 0; i < 9; i++) { xv[i] -= mean; q += xv[i] * xv[i]; *lp<float>(LQ_XC + (lane + 64 * i) * 4) = xv[i]; }
      const float rstd = __builtin_amdgcn_rsqf(wsum(q) * (1.f / 576.f) + 1e-5f);
      flag_set(F_RSTD, __float_as_uint(rstd));
      flag_set(F_LN, 1);
      if (lane < 12) *lp<u32>(LQ_ST + lane * 4) = SENT;
      STAMP(2);
    } else if (w == 9) {
      silu96(cvec, min(s1_kr, 5), lane);
      flag_set(F_SC, 1);
      STAMP(2);
    } else if (w == 10) {
      poll_ready(rw, W_RDYA, lane, tag);
      flag_set(F_RDY, 1);
      STAMP(2);
    }
    BAR();
    STAMP(5);
    const int r = j - 9;
    if (j < 9) {
      const int fj = b, gu = w >= 6, kr = w - 6 * gu;
      while (rtime() - t0 < T_F) __builtin_amdgcn_s_sleep(2);
      const u32 slot = w * 12288;
      const char* gb = (const char*)(gu ? wu : wg) + kr * 96 * 9216 + fj * 128;
      v4u fd[8], fg[12];
      if (w < 9) dma_run((const char*)wd + fj * 64 * 1152 + w * 128, unit_row * 1152, unit_col, 8 * 1152, 0, 8, 0x40, slot + 4096, 0, 0, 4);
      dma_run(gb, unit_row * 9216, unit_col, 8 * 9216, 0, 4, 0x40, slot, 0, 0, 4);
      unit_frags<2>(slot + 4096, lane, fd);
      asm volatile("s_waitcnt lgkmcnt(0)" : "+v"(fd[0]), "+v"(fd[1]), "+v"(fd[2]), "+v"(fd[3]), "+v"(fd[4]), "+v"(fd[5]), "+v"(fd[6]), "+v"(fd[7])::"memory");
      dma_run(gb, unit_row * 9216, unit_col, 8 * 9216, 4, 12, 0x40, slot + 4096, 0, 0, 4);
      const int cp = 96 * kr + 2 * min(lane, 47);
      v2u mg, ms, mc;
      for (u32 s = 0; s < SPIN_G; s++) {
        CFENCE();
        mg = ld64(rw, W_B7 + cp * 4); ms = ld64(rw, W_B7 + 2304 + cp * 4); mc = ld64(rw, W_B7 + 4608 + cp * 4);
        const u32 m = umax3(umax3(mg.x, mg.y, ms.x), ms.y, max(mc.x, mc.y));
        if (__all(m != SENT)) break;
        __builtin_amdgcn_s_sleep(4);
      }
      VM0();
      STAMP(6);
      unit_frags<3>(slot, lane, fg);
      const float c0 = 1.f + __uint_as_float(mc.x), c1 = 1.f + __uint_as_float(mc.y);
      const u32 ax = LQ_AX + w * 640;
      if (lane < 48) {
        *lp<u32>(ax + 208 + lane * 4) = pk(c0, c1);
        *lp<u32>(ax + 416 + lane * 4) = pk(__uint_as_float(ms.x), __uint_as_float(ms.y));
      }
      const f2 xc = *lp<f2>(LQ_XC + cp * 4);
      v2u pa[4];
      for (u32 s = 0; s < SPIN_G; s++) {
        CFENCE();
#pragma unroll
        for (int i = 0; i < 4; i++) pa[i] = ld64(rw, W_B6 + i * 2304 + cp * 4);
        const u32 m = umax3(umax3(umax3(pa[0].x, pa[0].y, pa[1].x), pa[1].y, pa[2].x), pa[2].y, max(pa[3].x, pa[3].y));
        if (__all(m != SENT)) break;
      }
      STAMP(7);
      const float a0 = (__uint_as_float(pa[0].x) + __uint_as_float(pa[1].x)) + (__uint_as_float(pa[2].x) + __uint_as_float(pa[3].x));
      const float a1 = (__uint_as_float(pa[0].y) + __uint_as_float(pa[1].y)) + (__uint_as_float(pa[2].y) + __uint_as_float(pa[3].y));
      const float y0 = xc.x + __uint_as_float(mg.x) * a0, y1 = xc.y + __uint_as_float(mg.y) * a1;
      if (lane < 48) *lp<u32>(ax + lane * 4) = pk(y0 * c0, y1 * c1);
      CFENCE();
      v4u af[3];
#pragma unroll
      for (int s = 0; s < 3; s++) af[s] = *lp<v4u>(ax + min(lane & 3, 2) * 208 + (32 * s + 8 * g4) * 2);
      if (!gu) {
        const float s1 = wsum(lane < 48 ? y0 + y1 : 0.f), s2 = wsum(lane < 48 ? y0 * y0 + y1 * y1 : 0.f);
        if (lane == 0) *(volatile LAS v2u*)lp<v2u>(LQ_ST + kr * 8) = v2u{__float_as_uint(s1), __float_as_uint(s2)};
      }
      f4 acc[4] = {};
#pragma unroll
      for (int s = 0; s < 3; s++)
#pragma unroll
        for (int t = 0; t < 4; t++) acc[t] = mfma16(af[s], fg[s * 4 + t], acc[t]);
      v4u q0, q1, q2;
      for (u32 s = 0; s < SPIN_L; s++) {
        q0 = *(volatile LAS v4u*)lp<v4u>(LQ_ST); q1 = *(volatile LAS v4u*)lp<v4u>(LQ_ST + 16); q2 = *(volatile LAS v4u*)lp<v4u>(LQ_ST + 32);
        if (__builtin_amdgcn_readfirstlane(vmax(vmax(vmax(0, q0), q1), q2)) != SENT) break;
      }
      const float mu = ((__uint_as_float(q0.x) + __uint_as_float(q0.z)) + (__uint_as_float(q1.x) + __uint_as_float(q1.z)) + (__uint_as_float(q2.x) + __uint_as_float(q2.z))) * (1.f / 576.f);
      const float e2 = ((__uint_as_float(q0.y) + __uint_as_float(q0.w)) + (__uint_as_float(q1.y) + __uint_as_float(q1.w)) + (__uint_as_float(q2.y) + __uint_as_float(q2.w))) * (1.f / 576.f);
      const float rstd = __builtin_amdgcn_rsqf(fmaxf(e2 - mu * mu, 0.f) + 1e-5f);
      const float vu = sel4(g4, acc[0].x, acc[1].x, acc[2].x, acc[3].x), v1 = sel4(g4, acc[0].y, acc[1].y, acc[2].y, acc[3].y),
                  v2 = sel4(g4, acc[0].z, acc[1].z, acc[2].z, acc[3].z);
      *lp<float>(LQ_GV + (w * 64 + lane) * 4) = rstd * (vu - mu * v1) + v2;
      BAR();
      STAMP(8);
      if (w < 9) {
        float gs = 0.f, us = 0.f;
#pragma unroll
        for (int i = 0; i < 6; i++) { gs += *lp<float>(LQ_GV + (i * 64 + lane) * 4); us += *lp<float>(LQ_GV + ((6 + i) * 64 + lane) * 4); }
        const float z = gs * (1.f + 0.044715f * gs * gs) * (-2.f * 0.7978845608028654f * 1.4426950408889634f);
        const float fv = gs * __builtin_amdgcn_rcpf(1.f + __builtin_amdgcn_exp2f(z)) * us;
        *lp<u16>(ax + lane * 2) = (u16)pk(fv, fv);
        CFENCE();
        f4 o[4] = {};
#pragma unroll
        for (int s = 0; s < 2; s++) {
          const v4u a = *lp<v4u>(ax + (32 * s + 8 * g4) * 2);
#pragma unroll
          for (int t = 0; t < 4; t++) o[t] = mfma16(a, fd[s * 4 + t], o[t]);
        }
        st32(rw, W_B8 + ((4 * w + g4) * 72 + fj) * 64 + c15 * 4, __float_as_uint(sel4(g4, o[0].x, o[1].x, o[2].x, o[3].x)));
        STAMP(9);
      }
    } else if (r < 4 || (r == 4 && !(xg & 1))) {
      const int kq = xg >> 1, cb = r < 4 ? 2 * r + (xg & 1) : 8;
      if (w < 8) {
        while (rtime() - t0 < T_O) __builtin_amdgcn_s_sleep(2);
        const int h = 8 * kq + w;
        dma_run((const char*)wo + h * 128 * 1152 + cb * 128, unit_row * 1152, unit_col, 8 * 1152, 0, 16, 0x40, w * 16384, 0, 0, 4);
        VM0();
        STAMP(6);
        v4u f[16];
        unit_frags<4>(w * 16384, lane, f);
        const u32 ao = W_B5 + (h * 4 + (lane & 3)) * 256 + g4 * 16;
        v4u a[4];
        for (u32 s = 0; s < SPIN_G; s++) {
          CFENCE();
#pragma unroll
          for (int i = 0; i < 4; i++) a[i] = ld128(rw, ao + i * 64);
          u32 m = 0;
#pragma unroll
          for (int i = 0; i < 4; i++) m = vmax(m, a[i]);
          if (__all(m != SENT)) break;
        }
        STAMP(7);
        f4 acc[4] = {};
#pragma unroll
        for (int s = 0; s < 4; s++)
#pragma unroll
          for (int t = 0; t < 4; t++) acc[t] = mfma16(a[s], f[s * 4 + t], acc[t]);
        float t4[4];
#pragma unroll
        for (int t = 0; t < 4; t++) t4[t] = (acc[t].x + acc[t].y) + (acc[t].z + acc[t].w);
        *lp<float>(LQ_GV + (w * 64 + lane) * 4) = sel4(g4, t4[0], t4[1], t4[2], t4[3]);
      }
      BAR();
      if (w == 8) {
        float v = 0.f;
#pragma unroll
        for (int i = 0; i < 8; i++) v += *lp<float>(LQ_GV + (i * 64 + lane) * 4);
        st32(rw, W_B6 + kq * 2304 + (64 * cb + lane) * 4, __float_as_uint(v));
        STAMP(8);
      }
    } else if (r < 6 || xg < 6) {
      const int mi = r == 4 ? (xg >> 1) : r == 5 ? 4 + xg : 12 + xg;
      const int ci = w >= 6, kr = w - 6 * ci;
      silu96(cvec, kr, lane);
      while (rtime() - t0 < T_M) __builtin_amdgcn_s_sleep(2);
      dma_run((const char*)wmod + kr * 96 * 6912 + (18 + 2 * mi + ci) * 128, unit_row * 6912, unit_col, 8 * 6912, 0, 12, 0x40, w * 12288, 0, 0, 4);
      VM0();
      STAMP(6);
      v4u f[12];
      unit_frags<3>(w * 12288, lane, f);
      f4 acc[4] = {};
#pragma unroll
      for (int s = 0; s < 3; s++) {
        const v4u a = *lp<v4u>(LQ_SC + (96 * kr + 32 * s + 8 * g4) * 2);
#pragma unroll
        for (int t = 0; t < 4; t++) acc[t] = mfma16(a, f[s * 4 + t], acc[t]);
      }
      *lp<float>(LQ_GV + (w * 64 + lane) * 4) = sel4(g4, acc[0].x, acc[1].x, acc[2].x, acc[3].x);
      BAR();
      if (w < 2) {
        float v = 0.f;
#pragma unroll
        for (int i = 0; i < 6; i++) v += *lp<float>(LQ_GV + ((6 * w + i) * 64 + lane) * 4);
        st32(rw, W_B7 + ((2 * mi + w) * 64 + lane) * 4, pk(v, v) & 0xFFFF0000u);
        STAMP(7);
        const int task = 2 * mi + w;
        const int col = 16 * task + 4 * (lane & 3) + g4;
        const float xv = bflo(((const GAS u16*)x)[col]);
        u32 ga, gbv, p0, p1, p2, p3;
        for (u32 s = 0; s < SPIN_G; s++) {
          CFENCE();
          ga = ld32(rw, W_B7 + col * 4); gbv = ld32(rw, W_B7 + 6912 + col * 4);
          p0 = ld32(rw, W_B6 + col * 4); p1 = ld32(rw, W_B6 + 2304 + col * 4); p2 = ld32(rw, W_B6 + 4608 + col * 4); p3 = ld32(rw, W_B6 + 6912 + col * 4);
          if (__all(umax3(umax3(ga, gbv, p0), p1, max(p2, p3)) != SENT)) break;
          __builtin_amdgcn_s_sleep(4);
        }
        const float x2 = xv + __uint_as_float(ga) * ((__uint_as_float(p0) + __uint_as_float(p1)) + (__uint_as_float(p2) + __uint_as_float(p3)));
        const u32 bo = W_B8 + task * 4608 + lane * 16;
        v4u a[5];
        for (u32 s = 0; s < SPIN_G; s++) {
          CFENCE();
#pragma unroll
          for (int i = 0; i < 4; i++) a[i] = ld128(rw, bo + i * 1024);
          a[4] = ld128(rw, W_B8 + task * 4608 + 4096 + (lane & 31) * 16);
          u32 m = 0;
#pragma unroll
          for (int i = 0; i < 5; i++) m = vmax(m, a[i]);
          if (__all(m != SENT)) break;
        }
        STAMP(8);
        f4 sv = (__builtin_bit_cast(f4, a[0]) + __builtin_bit_cast(f4, a[1])) + (__builtin_bit_cast(f4, a[2]) + __builtin_bit_cast(f4, a[3]));
        if (lane < 32) sv += __builtin_bit_cast(f4, a[4]);
        float y = fold16(fold32(sv.x, sv.z), fold32(sv.y, sv.w));
        y += dppf<0x128>(y);
        y += dppf<0x124>(y);
        const float res = x2 + __uint_as_float(gbv) * y;
        if (!(lane & 12)) ((GAS u16*)out)[col] = (u16)pk(res, res);
        if (task == 0 && lane == 0) st64(rw, W_EPOCH, tag);
        STAMP(9);
      }
    }
  } else {
    const int r31 = lane & 31, half = lane >> 5;
    if (w < 4) {
      if (lane == 0) __hip_atomic_fetch_add(lp<u32>(L_FLAG + 4 * F_ISS), 1u, __ATOMIC_RELAXED, __HIP_MEMORY_SCOPE_WORKGROUP);
      flag_wait_sleep(F_RDY, 1);
      v4u qd[2];
      for (u32 s = 0; s < SPIN_G; s++) {
        CFENCE();
        qd[0] = ld128(rw, W_B3 + r31 * 1024 + (60 + half) * 16);
        qd[1] = ld128(rw, W_B3 + r31 * 1024 + (62 + half) * 16);
        if (__all(vmax(vmax(0, qd[0]), qd[1]) != SENT)) break;
      }
      VM0();
      STAMP(2);
      const u32 fr = ((r31 & 3) << 2) | ((r31 >> 2) & 3);
      const u32 arow = (32 * w + r31) * 1024;
      v4u a0[16];
#pragma unroll
      for (int s = 0; s < 16; s++) a0[s] = *lp<v4u>(arow + (s >> 3) * 256 + ((((2 * s + half) & 15) ^ fr) << 4));
      BAR();
      STAMP(3);
      f16v sc = {};
      const u32 qrow = LA_Q + r31 * LA_QS + half * 16;
#pragma unroll
      for (int s = 0; s < 32; s++) {
        const v4u av = s < 16 ? a0[s & 15] : *lp<v4u>(arow + (s >> 3) * 256 + ((((2 * s + half) & 15) ^ fr) << 4));
        const v4u bv = s < 30 ? *lp<v4u>(qrow + s * 32) : qd[s & 1];
        sc = mfma32(av, bv, sc);
      }
      float m = fmaxf(fmaxf(fmaxf(sc[0], sc[1]), fmaxf(sc[2], sc[3])), fmaxf(fmaxf(sc[4], sc[5]), fmaxf(sc[6], sc[7])));
      m = fmaxf(m, fmaxf(fmaxf(fmaxf(sc[8], sc[9]), fmaxf(sc[10], sc[11])), fmaxf(fmaxf(sc[12], sc[13]), fmaxf(sc[14], sc[15]))));
      { float a = m, c = m; swap32(a, c); m = fmaxf(a, c); }
      if (lane < 32) *lp<float>(LA_MX + (r31 * 4 + w) * 4) = m;
      BAR();
      STAMP(4);
      { const f4 mm = *lp<f4>(LA_MX + r31 * 16); m = fmaxf(fmaxf(mm.x, mm.y), fmaxf(mm.z, mm.w)); }
      u32 pw[8];
      float l = 0.f;
#pragma unroll
      for (int i = 0; i < 8; i++) {
        pw[i] = pk(__builtin_amdgcn_exp2f(sc[2 * i] - m), __builtin_amdgcn_exp2f(sc[2 * i + 1] - m));
        l = dot2(pw[i], 0x3F803F80u, l);
      }
      *lp<v4u>(LA_P + (2 * w) * 1024 + lane * 16) = v4u{pw[0], pw[1], pw[2], pw[3]};
      *lp<v4u>(LA_P + (2 * w + 1) * 1024 + lane * 16) = v4u{pw[4], pw[5], pw[6], pw[7]};
      l = fold32(l, l);
      if (lane < 32) *lp<float>(LA_LS + (r31 * 4 + w) * 4) = l;
      BAR();
      STAMP(5);
      v4u pf[8];
#pragma unroll
      for (int k = 0; k < 8; k++) pf[k] = *lp<v4u>(LA_P + k * 1024 + lane * 16);
      const u32 blk = (lane >> 4) & 1, q = (lane & 15) >> 2, pp = lane & 3;
      const u32 tb = (4 * half + q) * 1024 + 256 * w + 8 * (pp & 1);
      flag_wait(F_RDYB, 1);
#pragma unroll
      for (int t = 0; t < 4; t++) {
        const u32 ch = 4 * t + 2 * blk + (pp >> 1);
        const u32 ad0 = tb + ((ch ^ ((q << 2) | half)) << 4), ad1 = tb + 8192 + ((ch ^ ((q << 2) | (2 + half))) << 4);
        f16v o = {};
#pragma unroll
        for (int k = 0; k < 8; k++) o = mfma32(trd2(ad0 + k * 16384, ad1 + k * 16384), pf[k], o);
        u32 x0[4], x1[4];
#pragma unroll
        for (int d = 0; d < 4; d++) {
          auto rr = __builtin_amdgcn_permlane16_swap(pk(o[2 * d], o[2 * d + 1]), pk(o[8 + 2 * d], o[9 + 2 * d]), false, false);
          x0[d] = rr[0]; x1[d] = rr[1];
        }
        const u32 so = W_B4 + ((c15 * 16 + 4 * w + t) * 128 + sidx) * 64 + g4 * 16;
        st128(rw, so, v4u{x0[0], x0[1], x0[2], x0[3]});
        st128(rw, so + 16 * 16 * 128 * 64, v4u{x1[0], x1[1], x1[2], x1[3]});
      }
      STAMP(6);
    } else {
      if (w == 8) {
        const v4u sv = {SENT, SENT, SENT, SENT};
#pragma unroll
        for (int i = 0; i < 3; i++) st128(rw, W_SM + sidx * 3072 + i * 1024 + lane * 16, sv);
        VM0();
        if (lane == 0) st64(rw, W_RDYA + sidx * 64, tag);
#pragma nounroll
        for (int i = 0; i < 32; i++) st128(rw, W_B4 + sidx * 32768 + i * 1024 + lane * 16, sv);
        VM0();
        if (lane == 0) st64(rw, W_RDYB + sidx * 64, tag);
        STAMP(2);
        poll_ready(rw, W_RDYA, lane, tag);
        flag_set(F_RDY, 1);
        poll_ready(rw, W_RDYB, lane, tag);
        flag_set(F_RDYB, 1);
      }
      flag_wait_sleep(F_RDY, 1);
      flag_wait_sleep(F_ISS, 4);
      {
        const int h0 = 4 * (w - 4), ch = min(lane, 59);
        v4u a[4];
        for (u32 s = 0; s < SPIN_G; s++) {
          CFENCE();
#pragma unroll
          for (int i = 0; i < 4; i++) a[i] = ld128(rw, W_B3 + (h0 + i) * 1024 + ch * 16);
          u32 m = 0;
#pragma unroll
          for (int i = 0; i < 4; i++) m = vmax(m, a[i]);
          if (__all(m != SENT)) break;
        }
#pragma unroll
        for (int i = 0; i < 4; i++) *lp<v4u>(LA_Q + (h0 + i) * LA_QS + ch * 16) = a[i];
      }
      STAMP(3);
      BAR();
      if (w < 8) {
        const int t = w - 4;
        v4u wr[8];
        const GAS char* wb = (const GAS char*)wuv + (size_t)(128 * cq + 32 * t + 16 * (g4 & 1) + 4 * (g4 >> 1)) * 8192 + hh * 256 + c15 * 16;
#pragma unroll
        for (int i = 0; i < 8; i++) wr[i] = __builtin_nontemporal_load((const GAS v4u*)(wb + ((i & 3) + 8 * (i >> 2)) * 8192));
        BAR();
        BAR();
        v4u wf[8];
#pragma unroll
        for (int d = 0; d < 4; d++) {
          wf[2 * d] = v4u{__builtin_amdgcn_perm(wr[1][d], wr[0][d], 0x05040100u), __builtin_amdgcn_perm(wr[3][d], wr[2][d], 0x05040100u),
                          __builtin_amdgcn_perm(wr[5][d], wr[4][d], 0x05040100u), __builtin_amdgcn_perm(wr[7][d], wr[6][d], 0x05040100u)};
          wf[2 * d + 1] = v4u{__builtin_amdgcn_perm(wr[1][d], wr[0][d], 0x07060302u), __builtin_amdgcn_perm(wr[3][d], wr[2][d], 0x07060302u),
                              __builtin_amdgcn_perm(wr[5][d], wr[4][d], 0x07060302u), __builtin_amdgcn_perm(wr[7][d], wr[6][d], 0x07060302u)};
        }
        asm volatile("" : "+v"(wf[0]), "+v"(wf[1]), "+v"(wf[2]), "+v"(wf[3]), "+v"(wf[4]), "+v"(wf[5]), "+v"(wf[6]), "+v"(wf[7]));
        STAMP(4);
        flag_wait_sleep(F_GO, 1);
        STAMP(5);
        const u32 oo = W_B4 + ((hh * 16 + 4 * cq + t) * 128 + c15) * 64 + g4 * 16;
        float ac[8];
        for (u32 s = 0; s < SPIN_G; s++) {
          CFENCE();
          v4u a[8];
#pragma unroll
          for (int i = 0; i < 8; i++) a[i] = ld128(rw, oo + i * 1024);
          flag_wait(F_WR, 1);
          const v4u w0 = *lp<v4u>(LA_W + c15 * 32), w1 = *lp<v4u>(LA_W + c15 * 32 + 16);
          u32 m = 0;
#pragma unroll
          for (int e = 0; e < 8; e++) ac[e] = 0.f;
#pragma unroll
          for (int i = 0; i < 8; i++) {
            const u32 wl = i < 4 ? w0[i & 3] : w1[i & 3], wh = wl << 16;
            m = vmax(m, a[i]);
#pragma unroll
            for (int d = 0; d < 4; d++) { ac[2 * d] = dot2(a[i][d], wl, ac[2 * d]); ac[2 * d + 1] = dot2(a[i][d], wh, ac[2 * d + 1]); }
          }
          if (__all(m != SENT)) break;
        }
        STAMP(6);
        const v4u af = {pk(ac[0], ac[1]), pk(ac[2], ac[3]), pk(ac[4], ac[5]), pk(ac[6], ac[7])};
        float t8[8];
#pragma unroll
        for (int i = 0; i < 8; i++) { const f4 d = mfma16(af, wf[i], f4{0.f, 0.f, 0.f, 0.f}); t8[i] = (d.x + d.y) + (d.z + d.w); }
        const float u0 = fold16(fold32(t8[0], t8[4]), fold32(t8[2], t8[6])), u1 = fold16(fold32(t8[1], t8[5]), fold32(t8[3], t8[7]));
        *lp<f2>(LA_RED + t * 512 + lane * 8) = f2{u0, u1};
        u32 old = 0;
        if (lane == 0) old = __hip_atomic_fetch_add(lp<u32>(L_FLAG + 4 * F_CNT), 1u, __ATOMIC_RELAXED, __HIP_MEMORY_SCOPE_WORKGROUP);
        if (__builtin_amdgcn_readfirstlane(old) == 3) {
          CFENCE();
          const f2 r0 = *lp<f2>(LA_RED + lane * 8), r1 = *lp<f2>(LA_RED + 512 + lane * 8), r2 = *lp<f2>(LA_RED + 1024 + lane * 8), r3 = *lp<f2>(LA_RED + 1536 + lane * 8);
          const float il = __uint_as_float(flag_get(F_INVL));
          const f2 rs = (r0 + r1) + (r2 + r3);
          st32(rw, W_B5 + (hh * 4 + cq) * 256 + c15 * 16 + g4 * 4, pk(rs.x * il, rs.y * il));
        }
        STAMP(7);
      } else {
        BAR();
        BAR();
        if (w == 9) {
          const f4 mm = *lp<f4>(LA_MX + r31 * 16), ll = *lp<f4>(LA_LS + r31 * 16);
          const float m = fmaxf(fmaxf(mm.x, mm.y), fmaxf(mm.z, mm.w)), l = (ll.x + ll.y) + (ll.z + ll.w);
          if (lane < 32) st64(rw, W_ML + sidx * 256 + lane * 8, v2u{__float_as_uint(m), __float_as_uint(l)});
          STAMP(4);
        } else if (w == 8) {
          v2u a, c;
          for (u32 s = 0; s < SPIN_G; s++) {
            CFENCE();
            a = ld64(rw, W_ML + lane * 256 + hh * 8); c = ld64(rw, W_ML + (lane + 64) * 256 + hh * 8);
            if (__all(umax3(max(a.x, a.y), c.x, c.y) != SENT)) break;
            __builtin_amdgcn_s_sleep(1);
          }
          flag_set(F_GO, 1);
          STAMP(5);
          const float M = wmax(fmaxf(__uint_as_float(a.x), __uint_as_float(c.x)));
          const u32 e = pk(__builtin_amdgcn_exp2f(__uint_as_float(a.x) - M), __builtin_amdgcn_exp2f(__uint_as_float(c.x) - M));
          *lp<u32>(LA_W + c15 * 32 + g4 * 4) = e & 0xFFFFu;
          *lp<u32>(LA_W + c15 * 32 + 16 + g4 * 4) = e >> 16;
          const float L = wsum(bflo(e) * __uint_as_float(a.y) + bfhi(e) * __uint_as_float(c.y));
          flag_set(F_INVL, __float_as_uint(__builtin_amdgcn_rcpf(L)));
          flag_set(F_WR, 1);
          STAMP(6);
        }
      }
    }
  }
  STAMP_OUT();
}

extern "C" void kernel_launch(void* const* d_in, const int* in_sizes, int n_in, void* d_out, int out_size, void* d_ws, size_t ws_size,
                              hipStream_t stream) {
  fused_decode<<<256, 768, 0, stream>>>((const u16*)d_in[0], (const u16*)d_in[1], (const u16*)d_in[2], (const u16*)d_in[3], (const u16*)d_in[4],
                                        (const u16*)d_in[5], (const u16*)d_in[7], (const u16*)d_in[8], (const u16*)d_in[9], (const u16*)d_in[10],
                                        (const u16*)d_in[11], (u16*)d_out, (char*)d_ws);
}
```

```cpp
#define AMP 3
#define X_EPOCH
#include <hip/hip_runtime.h>
#include <stdint.h>

typedef unsigned u32;
typedef unsigned long long u64;
typedef unsigned short u16;
typedef u32 v4u __attribute__((ext_vector_type(4)));
typedef u32 v2u __attribute__((ext_vector_type(2)));
typedef float f4 __attribute__((ext_vector_type(4)));
typedef float f2 __attribute__((ext_vector_type(2)));
typedef float f16v __attribute__((ext_vector_type(16)));
typedef short s4 __attribute__((ext_vector_type(4)));
typedef __bf16 b8 __attribute__((ext_vector_type(8)));
typedef __bf16 b2 __attribute__((ext_vector_type(2)));
#define LAS __attribute__((address_space(3)))
#define GAS __attribute__((address_space(1)))
#define DI __device__ __forceinline__

#define A_N1 6
#define A_P1 2
#define T_F 0
#define T_O 0
#define T_M 0

constexpr u32 W_EPOCH = 0;
constexpr u32 W_RDYA = 4096, W_RDYB = 12288;
constexpr u32 W_B1 = 20480;
constexpr u32 W_SM = 49152;
constexpr u32 W_B2 = W_SM;
constexpr u32 W_B3 = W_SM + 49152;
constexpr u32 W_ML = W_SM + 81920;
constexpr u32 W_B5 = W_SM + 114688;
constexpr u32 W_B6 = W_SM + 147456;
constexpr u32 W_B7 = W_SM + 156672;
constexpr u32 W_B8 = W_SM + 165888;
constexpr u32 W_B4 = 524288;
constexpr u32 W_END = W_B4 + 4194304;
constexpr u32 W_STAMP = 8u << 20;
constexpr u32 SENT = 0xFFFFFFFFu;

constexpr u32 L_FLAG = 163776;
constexpr u32 LA_Q = 131072, LA_QS = 976;
constexpr u32 LA_P = 131072, LA_W = 139264, LA_RED = 139776;
constexpr u32 LA_MX = 162304, LA_LS = 162816;
constexpr u32 LQ_XC = 147456, LQ_SC = 149760, LQ_AX = 151040, LQ_GV = 158720, LQ_ST = 161792;
enum { F_LN = 0, F_SC = 1, F_RDY = 2, F_RSTD = 3, F_ISS = 4, F_GO = 5, F_WR = 6, F_INVL = 7, F_CNT = 8, F_RDYB = 9 };

constexpr u32 SPIN_G = 1u << 15, SPIN_L = 1u << 20;

DI u64 rtime() { return __builtin_amdgcn_s_memrealtime(); }
#define STAMP(i) do {} while (0)
#define STAMP_OUT() do {} while (0)

template <class T> DI LAS T* lp(u32 a) { return (LAS T*)(uintptr_t)a; }
DI float bflo(u32 v) { return __uint_as_float(v << 16); }
DI float bfhi(u32 v) { return __uint_as_float(v & 0xFFFF0000u); }
DI u32 pk(float a, float b) { f2 v = {a, b}; return __builtin_bit_cast(u32, __builtin_convertvector(v, b2)); }
DI f4 mfma16(v4u a, v4u b, f4 c) { return __builtin_amdgcn_mfma_f32_16x16x32_bf16(__builtin_bit_cast(b8, a), __builtin_bit_cast(b8, b), c, 0, 0, 0); }
DI f16v mfma32(v4u a, v4u b, f16v c) { return __builtin_amdgcn_mfma_f32_32x32x16_bf16(__builtin_bit_cast(b8, a), __builtin_bit_cast(b8, b), c, 0, 0, 0); }
DI float dot2(u32 a, u32 b, float c) { return __builtin_amdgcn_fdot2_f32_bf16(__builtin_bit_cast(b2, a), __builtin_bit_cast(b2, b), c, false); }
DI v2u trd(u32 a) { return __builtin_bit_cast(v2u, __builtin_amdgcn_ds_read_tr16_b64_v4i16(lp<s4>(a))); }
DI v4u trd2(u32 a0, u32 a1) { v2u x = trd(a0), y = trd(a1); return v4u{x.x, x.y, y.x, y.y}; }
template <int C> DI float dppf(float v) { return __uint_as_float(__builtin_amdgcn_update_dpp(0u, __float_as_uint(v), C, 0xF, 0xF, true)); }
DI void swap16(float& a, float& b) { auto r = __builtin_amdgcn_permlane16_swap(__float_as_uint(a), __float_as_uint(b), false, false); a = __uint_as_float(r[0]); b = __uint_as_float(r[1]); }
DI void swap32(float& a, float& b) { auto r = __builtin_amdgcn_permlane32_swap(__float_as_uint(a), __float_as_uint(b), false, false); a = __uint_as_float(r[0]); b = __uint_as_float(r[1]); }
DI float fold16(float a, float b) { swap16(a, b); return a + b; }
DI float fold32(float a, float b) { swap32(a, b); return a + b; }
DI float wsum(float v) { v += dppf<0x128>(v); v += dppf<0x124>(v); v += dppf<0x122>(v); v += dppf<0x121>(v); v = fold16(v, v); return fold32(v, v); }
DI float wmax(float v) {
  v = fmaxf(v, dppf<0x128>(v)); v = fmaxf(v, dppf<0x124>(v)); v = fmaxf(v, dppf<0x122>(v)); v = fmaxf(v, dppf<0x121>(v));
  float a = v, c = v; swap16(a, c); v = fmaxf(a, c); a = v; c = v; swap32(a, c); return fmaxf(a, c);
}
DI u32 umax3(u32 a, u32 b, u32 c) { return max(max(a, b), c); }
DI u32 vmax(u32 m, v4u v) { return umax3(umax3(m, v.x, v.y), v.z, v.w); }
#define CFENCE() asm volatile("" ::: "memory")
#define VM0() asm volatile("s_waitcnt vmcnt(0)" ::: "memory")
#define BAR() asm volatile("s_waitcnt lgkmcnt(0)\n\ts_barrier" ::: "memory")
typedef __amdgpu_buffer_rsrc_t rsrc_t;
DI v4u ld128(rsrc_t r, u32 o) { return __builtin_amdgcn_raw_buffer_load_b128(r, (int)o, 0, 16); }
DI v2u ld64(rsrc_t r, u32 o) { return __builtin_amdgcn_raw_buffer_load_b64(r, (int)o, 0, 16); }
DI u32 ld32(rsrc_t r, u32 o) { return __builtin_amdgcn_raw_buffer_load_b32(r, (int)o, 0, 16); }
DI void st128(rsrc_t r, u32 o, v4u v) { __builtin_amdgcn_raw_buffer_store_b128(v, r, (int)o, 0, 16); }
DI void st64(rsrc_t r, u32 o, v2u v) { __builtin_amdgcn_raw_buffer_store_b64(v, r, (int)o, 0, 16); }
DI void st32(rsrc_t r, u32 o, u32 v) { __builtin_amdgcn_raw_buffer_store_b32(v, r, (int)o, 0, 16); }

DI void flag_set(int f, u32 v) { CFENCE(); *(volatile LAS u32*)lp<u32>(L_FLAG + 4 * f) = v; CFENCE(); }
DI u32 flag_get(int f) { return *(volatile LAS u32*)lp<u32>(L_FLAG + 4 * f); }
DI void flag_wait(int f, u32 v) { for (u32 s = 0; s < SPIN_L; s++) { if (__builtin_amdgcn_readfirstlane(flag_get(f)) >= v) break; } CFENCE(); }
DI void flag_wait_sleep(int f, u32 v) { for (u32 s = 0; s < SPIN_L; s++) { if (__builtin_amdgcn_readfirstlane(flag_get(f)) >= v) break; __builtin_amdgcn_s_sleep(1); } CFENCE(); }

DI void dma1(u32 lds, u32 voff, const void* base) {
  asm volatile("s_mov_b32 m0, %0\n\ts_nop 0\n\tglobal_load_lds_dwordx4 %1, %2 nt" ::"s"(lds), "v"(voff), "s"(base) : "memory");
}
DI void dma_run(const void* base, u32 rowoff, u32 coloff, u32 step, int i0, int n, u32 mask, u32 lds, int n1, int p1, int p2) {
#pragma nounroll
  for (int i = i0; i < n; i++) {
    const u32 xr = (((i & 3) << 6) | (((i >> 2) & 3) << 4)) & mask;
    dma1(lds + (i - i0) * 1024, rowoff + (coloff ^ xr) + i * step, base);
    const int p = i < n1 ? p1 : p2;
    if (p == 2) asm volatile("s_waitcnt vmcnt(1)" ::: "memory");
    else if (p == 4) asm volatile("s_waitcnt vmcnt(3)" ::: "memory");
    else if (p == 8) asm volatile("s_waitcnt vmcnt(7)" ::: "memory");
  }
}
template <int NU> DI void unit_frags(u32 a, int lane, v4u (&f)[NU * 4]) {
  const u32 g = lane >> 4, q = (lane & 15) >> 2, pp = lane & 3;
  const u32 sw = (((q >> 1) & 1) << 1) | ((g & 1) << 2);
  const u32 base = a + (8 * g + q) * 128 + 8 * (pp & 1);
#pragma unroll
  for (int t = 0; t < 4; t++) {
    const u32 ad = base + (((2 * t + (pp >> 1)) ^ sw) << 4);
#pragma unroll
    for (int u = 0; u < NU; u++) f[u * 4 + t] = trd2(ad + u * 4096, ad + u * 4096 + 512);
  }
}
DI float sel4(int g, float a, float b, float c, float d) { return g == 0 ? a : g == 1 ? b : g == 2 ? c : d; }
DI float silu(float v) { return v * __builtin_amdgcn_rcpf(1.f + __builtin_amdgcn_exp2f(-1.4426950408889634f * v)); }
DI void silu96(const u16* c, int kr, int lane) {
  if (lane < 48) {
    const u32 v = ((const GAS u32*)c)[48 * kr + lane];
    *lp<u32>(LQ_SC + (48 * kr + lane) * 4) = pk(silu(bflo(v)), silu(bfhi(v)));
  }
}
DI void poll_ready(rsrc_t rw, u32 off, int lane, v2u tag) {
  for (u32 s = 0; s < SPIN_G; s++) {
    CFENCE();
    const v2u a = ld64(rw, off + lane * 64), c = ld64(rw, off + 4096 + lane * 64);
    const bool ok = (a.x == tag.x) & (a.y == tag.y) & (c.x == tag.x) & (c.y == tag.y);
    if (__all(ok)) break;
    __builtin_amdgcn_s_sleep(2);
  }
  CFENCE();
}

__global__ void __launch_bounds__(768, 3)
fused_decode(const u16* __restrict__ x, const u16* __restrict__ ckv, const u16* __restrict__ wq, const u16* __restrict__ wuk,
             const u16* __restrict__ wuv, const u16* __restrict__ wo, const u16* __restrict__ wg, const u16* __restrict__ wu,
             const u16* __restrict__ wd, const u16* __restrict__ cvec, const u16* __restrict__ wmod, u16* __restrict__ out, char* ws) {
  __shared__ __attribute__((aligned(16))) char smem[163840];
  const int tid = threadIdx.x, lane = tid & 63;
  const int w = __builtin_amdgcn_readfirstlane(tid >> 6);
  const int b = blockIdx.x, xg = b & 7, j = b >> 3;
  const bool isQ = j < 16;
  const u64 t0 = rtime();
  asm volatile("" ::"v"((LAS char*)smem));
  *lp<u32>(L_FLAG + (tid & 15) * 4) = 0;
  asm volatile("s_waitcnt lgkmcnt(0)\n\ts_barrier" ::"s"(x), "s"(ckv), "s"(wq), "s"(wuk), "s"(wuv), "s"(wo), "s"(wg), "s"(wu), "s"(wd), "s"(cvec), "s"(wmod), "s"(out), "s"(ws) : "memory");
  const rsrc_t rw = __builtin_amdgcn_make_buffer_rsrc(ws, 0, (int)W_END, 0x00020000);
  if (w == 11 && lane == 0) __hip_atomic_store((GAS u64*)(ws + W_STAMP) + b, t0, __ATOMIC_RELAXED, __HIP_MEMORY_SCOPE_AGENT);
  STAMP(9);
  v2u ep = {0, 0};
  if (isQ ? (w < 4 || w == 10) : w == 8) ep = ld64(rw, W_EPOCH);

  const int g4 = lane >> 4, c15 = lane & 15;
  const u32 unit_row = lane >> 3, unit_col = ((lane & 7) ^ (((lane >> 4) & 1) << 1)) << 4;
  const int s1_cb = b % 18, s1_kr = b / 18;
  const int s2_cb = j >> 1, s2_kr = 3 * (j & 1) + (w - 1);
  const int hh = 4 * xg + ((j >> 2) & 3), cq = j & 3;
  const int sidx = (j - 16) * 8 + xg;
  {
    const int kind = !isQ ? 0 : w == 0 ? 1 : w < 4 ? 2 : 3;
    const char* p = (const char*)(kind == 0 ? ckv : kind == 1 ? wmod : kind == 2 ? wq : wuk);
    const u32 rs = kind == 0 ? 1024 : kind == 1 ? 6912 : 8192;
    const u32 row0 = kind == 0 ? 128 * sidx + 32 * w : kind == 1 ? 96 * s1_kr : kind == 2 ? 96 * s2_kr : 128 * cq + 32 * (w - 4);
    const u32 col0 = kind == 0 ? 0 : kind == 1 ? s1_cb * 128 : kind == 2 ? (8 * xg + s2_cb) * 128 : hh * 256;
    const u32 sh = kind == 0 ? 6 : kind == 3 ? 4 : 3;
    const u32 m1 = kind == 0 ? 0 : kind == 3 ? 0x30 : 0x20, s1 = kind == 3 ? 0 : 1;
    const u32 mask = kind == 0 ? 0xF0 : kind == 3 ? 0xC0 : 0x40;
    const u32 lds = w * (kind == 0 ? 32768 : 12288);
    const int n = kind == 0 ? (w < 4 ? 32 : 0) : kind == 1 ? (b < 108 ? 12 : 0) : kind == 2 ? 12 : (w < 8 ? 8 : 0);
    const int n1 = kind == 0 ? A_N1 : 0, p2 = kind == 0 ? 8 : kind == 1 ? 0 : 4;
    const u32 rowoff = (lane >> sh) * rs, coloff = ((lane << 4) & ((16u << sh) - 1)) ^ ((lane << s1) & m1);
    dma_run(p + (row0 * rs + col0), rowoff, coloff, rs << (6 - sh), 0, n, mask, lds, n1, A_P1, p2);
  }
  STAMP(1);
  v2u tag;
  {
    const u64 e = (((u64)ep.y << 32) | ep.x) + 1;
    tag = v2u{(u32)e, (u32)(e >> 32)};
  }

  if (isQ) {
    if (w == 0) {
      if (b < 108) {
        VM0();
        STAMP(2);
        v4u f[12];
        unit_frags<3>(0, lane, f);
        flag_wait(F_SC, 1);
        f4 acc[4] = {};
#pragma unroll
        for (int s = 0; s < 3; s++) {
          const v4u a = *lp<v4u>(LQ_SC + (96 * s1_kr + 32 * s + 8 * g4) * 2);
#pragma unroll
          for (int t = 0; t < 4; t++) acc[t] = mfma16(a, f[s * 4 + t], acc[t]);
        }
        float v = sel4(g4, acc[0].x, acc[1].x, acc[2].x, acc[3].x);
        const int isc = s1_cb >= 9, cb = s1_cb - 9 * isc;
        if (isc) {
          flag_wait(F_LN, 1);
          const float ln = *lp<float>(LQ_XC + (64 * cb + lane) * 4) * __uint_as_float(flag_get(F_RSTD));
          v = ln * (v + (s1_kr == 0 ? 1.f : 0.f));
        }
        const u32 pr = pk(v, dppf<0xB1>(v));
        if (!(lane & 1)) st64(rw, W_B1 + (6 * isc + s1_kr) * 2304 + (32 * cb + (lane >> 1)) * 8, v2u{pr, tag.x});
        STAMP(3);
      }
    } else if (w < 4) {
      VM0();
      STAMP(2);
      v4u f[12];
      unit_frags<3>(w * 12288, lane, f);
      const u32 row = 3 * (c15 >> 2) + min(c15 & 3, 2);
      const u32 ao = W_B1 + row * 2304 + (96 * s2_kr + 8 * g4) * 4;
      v4u a[6];
      for (u32 s = 0; s < SPIN_G; s++) {
        CFENCE();
#pragma unroll
        for (int i = 0; i < 6; i++) a[i] = ld128(rw, ao + (i >> 1) * 128 + (i & 1) * 16);
        bool ok = true;
#pragma unroll
        for (int i = 0; i < 6; i++) ok &= (a[i].y == tag.x) & (a[i].w == tag.x);
        if (__all(ok)) break;
      }
      STAMP(3);
      f4 acc[4] = {};
#pragma unroll
      for (int s = 0; s < 3; s++) {
        const v4u af = v4u{a[2 * s].x, a[2 * s].z, a[2 * s + 1].x, a[2 * s + 1].z};
#pragma unroll
        for (int t = 0; t < 4; t++) acc[t] = mfma16(af, f[s * 4 + t], acc[t]);
      }
      float t4[4];
#pragma unroll
      for (int t = 0; t < 4; t++) t4[t] = acc[t].x + acc[t].y + acc[t].z;
      const float v = fold16(fold32(t4[0], t4[2]), fold32(t4[1], t4[3]));
      const u32 pr = pk(v, dppf<0xB1>(v));
      flag_wait(F_RDY, 1);
      if (!(lane & 1)) st32(rw, W_B2 + ((4 * xg + (s2_cb >> 1)) * 6 + s2_kr) * 256 + (s2_cb & 1) * 128 + lane * 2, pr);
      STAMP(4);
    } else if (w < 8) {
      VM0();
      STAMP(2);
      v4u f[8];
#pragma unroll
      for (int t = 0; t < 2; t++)
#pragma unroll
        for (int s = 0; s < 4; s++) f[t * 4 + s] = *lp<v4u>(w * 12288 + (16 * t + c15) * 256 + (((4 * s + g4) ^ c15) << 4));
      flag_wait_sleep(F_RDY, 1);
      const u32 p = 3 * ((c15 >> 2) & 1) + min(c15 & 3, 2);
      const u32 ao = W_B2 + (hh * 6 + p) * 256 + g4 * 16;
      v4u a[4];
      for (u32 s = 0; s < SPIN_G; s++) {
        CFENCE();
#pragma unroll
        for (int i = 0; i < 4; i++) a[i] = ld128(rw, ao + i * 64);
        u32 m = 0;
#pragma unroll
        for (int i = 0; i < 4; i++) m = vmax(m, a[i]);
        if (__all(m != SENT)) break;
      }
      STAMP(3);
      f4 acc[2] = {};
#pragma unroll
      for (int s = 0; s < 4; s++)
#pragma unroll
        for (int t = 0; t < 2; t++) acc[t] = mfma16(a[s], f[t * 4 + s], acc[t]);
      const float v = fold16(acc[0].x + acc[0].y + acc[0].z, acc[1].x + acc[1].y + acc[1].z) * (0.08838834764831845f * 1.4426950408889634f);
      const u32 pr = pk(v, dppf<0xB1>(v));
      if (!(lane & 33)) st32(rw, W_B3 + hh * 1024 + (128 * cq + 32 * (w - 4) + lane) * 2, pr);
      STAMP(4);
    } else if (w == 8) {
      float xv[9], s = 0.f;
#pragma unroll
      for (int i = 0; i < 9; i++) xv[i] = bflo(((const GAS u16*)x)[lane + 64 * i]);
#pragma unroll
      for (int i = 0; i < 9; i++) s += xv[i];
      const float mean = wsum(s) * (1.f / 576.f);
      float q = 0.f;
#pragma unroll
      for (int i = 0; i < 9; i++) { xv[i] -= mean; q += xv[i] * xv[i]; *lp<float>(LQ_XC + (lane + 64 * i) * 4) = xv[i]; }
      const float rstd = __builtin_amdgcn_rsqf(wsum(q) * (1.f / 576.f) + 1e-5f);
      flag_set(F_RSTD, __float_as_uint(rstd));
      flag_set(F_LN, 1);
      if (lane < 12) *lp<u32>(LQ_ST + lane * 4) = SENT;
      STAMP(2);
    } else if (w == 9) {
      silu96(cvec, min(s1_kr, 5), lane);
      flag_set(F_SC, 1);
      STAMP(2);
    } else if (w == 10) {
      poll_ready(rw, W_RDYA, lane, tag);
      flag_set(F_RDY, 1);
      STAMP(2);
    }
    BAR();
    STAMP(5);
    const int r = j - 9;
    if (j < 9) {
      const int fj = b, gu = w >= 6, kr = w - 6 * gu;
      while (rtime() - t0 < T_F) __builtin_amdgcn_s_sleep(2);
      const u32 slot = w * 12288;
      const char* gb = (const char*)(gu ? wu : wg) + kr * 96 * 9216 + fj * 128;
      v4u fd[8], fg[12];
      if (w < 9) dma_run((const char*)wd + fj * 64 * 1152 + w * 128, unit_row * 1152, unit_col, 8 * 1152, 0, 8, 0x40, slot + 4096, 0, 0, 4);
      dma_run(gb, unit_row * 9216, unit_col, 8 * 9216, 0, 4, 0x40, slot, 0, 0, 4);
      unit_frags<2>(slot + 4096, lane, fd);
      asm volatile("s_waitcnt lgkmcnt(0)" : "+v"(fd[0]), "+v"(fd[1]), "+v"(fd[2]), "+v"(fd[3]), "+v"(fd[4]), "+v"(fd[5]), "+v"(fd[6]), "+v"(fd[7])::"memory");
      dma_run(gb, unit_row * 9216, unit_col, 8 * 9216, 4, 12, 0x40, slot + 4096, 0, 0, 4);
      const int cp = 96 * kr + 2 * min(lane, 47);
      v2u mg, ms, mc;
      for (u32 s = 0; s < SPIN_G; s++) {
        CFENCE();
        mg = ld64(rw, W_B7 + cp * 4); ms = ld64(rw, W_B7 + 2304 + cp * 4); mc = ld64(rw, W_B7 + 4608 + cp * 4);
        const u32 m = umax3(umax3(mg.x, mg.y, ms.x), ms.y, max(mc.x, mc.y));
        if (__all(m != SENT)) break;
        __builtin_amdgcn_s_sleep(4);
      }
      VM0();
      STAMP(6);
      unit_frags<3>(slot, lane, fg);
      const float c0 = 1.f + __uint_as_float(mc.x), c1 = 1.f + __uint_as_float(mc.y);
      const u32 ax = LQ_AX + w * 640;
      if (lane < 48) {
        *lp<u32>(ax + 208 + lane * 4) = pk(c0, c1);
        *lp<u32>(ax + 416 + lane * 4) = pk(__uint_as_float(ms.x), __uint_as_float(ms.y));
      }
      const f2 xc = *lp<f2>(LQ_XC + cp * 4);
      v2u pa[4];
      for (u32 s = 0; s < SPIN_G; s++) {
        CFENCE();
#pragma unroll
        for (int i = 0; i < 4; i++) pa[i] = ld64(rw, W_B6 + i * 2304 + cp * 4);
        const u32 m = umax3(umax3(umax3(pa[0].x, pa[0].y, pa[1].x), pa[1].y, pa[2].x), pa[2].y, max(pa[3].x, pa[3].y));
        if (__all(m != SENT)) break;
      }
      STAMP(7);
      const float a0 = (__uint_as_float(pa[0].x) + __uint_as_float(pa[1].x)) + (__uint_as_float(pa[2].x) + __uint_as_float(pa[3].x));
      const float a1 = (__uint_as_float(pa[0].y) + __uint_as_float(pa[1].y)) + (__uint_as_float(pa[2].y) + __uint_as_float(pa[3].y));
      const float y0 = xc.x + __uint_as_float(mg.x) * a0, y1 = xc.y + __uint_as_float(mg.y) * a1;
      if (lane < 48) *lp<u32>(ax + lane * 4) = pk(y0 * c0, y1 * c1);
      CFENCE();
      v4u af[3];
#pragma unroll
      for (int s = 0; s < 3; s++) af[s] = *lp<v4u>(ax + min(lane & 3, 2) * 208 + (32 * s + 8 * g4) * 2);
      if (!gu) {
        const float s1 = wsum(lane < 48 ? y0 + y1 : 0.f), s2 = wsum(lane < 48 ? y0 * y0 + y1 * y1 : 0.f);
        if (lane == 0) *(volatile LAS v2u*)lp<v2u>(LQ_ST + kr * 8) = v2u{__float_as_uint(s1), __float_as_uint(s2)};
      }
      f4 acc[4] = {};
#pragma unroll
      for (int s = 0; s < 3; s++)
#pragma unroll
        for (int t = 0; t < 4; t++) acc[t] = mfma16(af[s], fg[s * 4 + t], acc[t]);
      v4u q0, q1, q2;
      for (u32 s = 0; s < SPIN_L; s++) {
        q0 = *(volatile LAS v4u*)lp<v4u>(LQ_ST); q1 = *(volatile LAS v4u*)lp<v4u>(LQ_ST + 16); q2 = *(volatile LAS v4u*)lp<v4u>(LQ_ST + 32);
        if (__builtin_amdgcn_readfirstlane(vmax(vmax(vmax(0, q0), q1), q2)) != SENT) break;
      }
      const float mu = ((__uint_as_float(q0.x) + __uint_as_float(q0.z)) + (__uint_as_float(q1.x) + __uint_as_float(q1.z)) + (__uint_as_float(q2.x) + __uint_as_float(q2.z))) * (1.f / 576.f);
      const float e2 = ((__uint_as_float(q0.y) + __uint_as_float(q0.w)) + (__uint_as_float(q1.y) + __uint_as_float(q1.w)) + (__uint_as_float(q2.y) + __uint_as_float(q2.w))) * (1.f / 576.f);
      const float rstd = __builtin_amdgcn_rsqf(fmaxf(e2 - mu * mu, 0.f) + 1e-5f);
      const float vu = sel4(g4, acc[0].x, acc[1].x, acc[2].x, acc[3].x), v1 = sel4(g4, acc[0].y, acc[1].y, acc[2].y, acc[3].y),
                  v2 = sel4(g4, acc[0].z, acc[1].z, acc[2].z, acc[3].z);
      *lp<float>(LQ_GV + (w * 64 + lane) * 4) = rstd * (vu - mu * v1) + v2;
      BAR();
      STAMP(8);
      if (w < 9) {
        float gs = 0.f, us = 0.f;
#pragma unroll
        for (int i = 0; i < 6; i++) { gs += *lp<float>(LQ_GV + (i * 64 + lane) * 4); us += *lp<float>(LQ_GV + ((6 + i) * 64 + lane) * 4); }
        const float z = gs * (1.f + 0.044715f * gs * gs) * (-2.f * 0.7978845608028654f * 1.4426950408889634f);
        const float fv = gs * __builtin_amdgcn_rcpf(1.f + __builtin_amdgcn_exp2f(z)) * us;
        *lp<u16>(ax + lane * 2) = (u16)pk(fv, fv);
        CFENCE();
        f4 o[4] = {};
#pragma unroll
        for (int s = 0; s < 2; s++) {
          const v4u a = *lp<v4u>(ax + (32 * s + 8 * g4) * 2);
#pragma unroll
          for (int t = 0; t < 4; t++) o[t] = mfma16(a, fd[s * 4 + t], o[t]);
        }
        st32(rw, W_B8 + ((4 * w + g4) * 72 + fj) * 64 + c15 * 4, __float_as_uint(sel4(g4, o[0].x, o[1].x, o[2].x, o[3].x)));
        STAMP(9);
      }
    } else if (r < 4 || (r == 4 && !(xg & 1))) {
      const int kq = xg >> 1, cb = r < 4 ? 2 * r + (xg & 1) : 8;
      if (w < 8) {
        while (rtime() - t0 < T_O) __builtin_amdgcn_s_sleep(2);
        const int h = 8 * kq + w;
        dma_run((const char*)wo + h * 128 * 1152 + cb * 128, unit_row * 1152, unit_col, 8 * 1152, 0, 16, 0x40, w * 16384, 0, 0, 4);
        VM0();
        STAMP(6);
        v4u f[16];
        unit_frags<4>(w * 16384, lane, f);
        const u32 ao = W_B5 + (h * 4 + (lane & 3)) * 256 + g4 * 16;
        v4u a[4];
        for (u32 s = 0; s < SPIN_G; s++) {
          CFENCE();
#pragma unroll
          for (int i = 0; i < 4; i++) a[i] = ld128(rw, ao + i * 64);
          u32 m = 0;
#pragma unroll
          for (int i = 0; i < 4; i++) m = vmax(m, a[i]);
          if (__all(m != SENT)) break;
        }
        STAMP(7);
        f4 acc[4] = {};
#pragma unroll
        for (int s = 0; s < 4; s++)
#pragma unroll
          for (int t = 0; t < 4; t++) acc[t] = mfma16(a[s], f[s * 4 + t], acc[t]);
        float t4[4];
#pragma unroll
        for (int t = 0; t < 4; t++) t4[t] = (acc[t].x + acc[t].y) + (acc[t].z + acc[t].w);
        *lp<float>(LQ_GV + (w * 64 + lane) * 4) = sel4(g4, t4[0], t4[1], t4[2], t4[3]);
      }
      BAR();
      if (w == 8) {
        float v = 0.f;
#pragma unroll
        for (int i = 0; i < 8; i++) v += *lp<float>(LQ_GV + (i * 64 + lane) * 4);
        st32(rw, W_B6 + kq * 2304 + (64 * cb + lane) * 4, __float_as_uint(v));
        STAMP(8);
      }
    } else if (r < 6 || xg < 6) {
      const int mi = r == 4 ? (xg >> 1) : r == 5 ? 4 + xg : 12 + xg;
      const int ci = w >= 6, kr = w - 6 * ci;
      silu96(cvec, kr, lane);
      while (rtime() - t0 < T_M) __builtin_amdgcn_s_sleep(2);
      dma_run((const char*)wmod + kr * 96 * 6912 + (18 + 2 * mi + ci) * 128, unit_row * 6912, unit_col, 8 * 6912, 0, 12, 0x40, w * 12288, 0, 0, 4);
      VM0();
      STAMP(6);
      v4u f[12];
      unit_frags<3>(w * 12288, lane, f);
      f4 acc[4] = {};
#pragma unroll
      for (int s = 0; s < 3; s++) {
        const v4u a = *lp<v4u>(LQ_SC + (96 * kr + 32 * s + 8 * g4) * 2);
#pragma unroll
        for (int t = 0; t < 4; t++) acc[t] = mfma16(a, f[s * 4 + t], acc[t]);
      }
      *lp<float>(LQ_GV + (w * 64 + lane) * 4) = sel4(g4, acc[0].x, acc[1].x, acc[2].x, acc[3].x);
      BAR();
      if (w < 2) {
        float v = 0.f;
#pragma unroll
        for (int i = 0; i < 6; i++) v += *lp<float>(LQ_GV + ((6 * w + i) * 64 + lane) * 4);
        st32(rw, W_B7 + ((2 * mi + w) * 64 + lane) * 4, pk(v, v) & 0xFFFF0000u);
        STAMP(7);
        const int task = 2 * mi + w;
        const int col = 16 * task + 4 * (lane & 3) + g4;
        const float xv = bflo(((const GAS u16*)x)[col]);
        u32 ga, gbv, p0, p1, p2, p3;
        for (u32 s = 0; s < SPIN_G; s++) {
          CFENCE();
          ga = ld32(rw, W_B7 + col * 4); gbv = ld32(rw, W_B7 + 6912 + col * 4);
          p0 = ld32(rw, W_B6 + col * 4); p1 = ld32(rw, W_B6 + 2304 + col * 4); p2 = ld32(rw, W_B6 + 4608 + col * 4); p3 = ld32(rw, W_B6 + 6912 + col * 4);
          if (__all(umax3(umax3(ga, gbv, p0), p1, max(p2, p3)) != SENT)) break;
          __builtin_amdgcn_s_sleep(4);
        }
        const float x2 = xv + __uint_as_float(ga) * ((__uint_as_float(p0) + __uint_as_float(p1)) + (__uint_as_float(p2) + __uint_as_float(p3)));
        const u32 bo = W_B8 + task * 4608 + lane * 16;
        v4u a[5];
        for (u32 s = 0; s < SPIN_G; s++) {
          CFENCE();
#pragma unroll
          for (int i = 0; i < 4; i++) a[i] = ld128(rw, bo + i * 1024);
          a[4] = ld128(rw, W_B8 + task * 4608 + 4096 + (lane & 31) * 16);
          u32 m = 0;
#pragma unroll
          for (int i = 0; i < 5; i++) m = vmax(m, a[i]);
          if (__all(m != SENT)) break;
        }
        STAMP(8);
        f4 sv = (__builtin_bit_cast(f4, a[0]) + __builtin_bit_cast(f4, a[1])) + (__builtin_bit_cast(f4, a[2]) + __builtin_bit_cast(f4, a[3]));
        if (lane < 32) sv += __builtin_bit_cast(f4, a[4]);
        float y = fold16(fold32(sv.x, sv.z), fold32(sv.y, sv.w));
        y += dppf<0x128>(y);
        y += dppf<0x124>(y);
        const float res = x2 + __uint_as_float(gbv) * y;
        if (!(lane & 12)) ((GAS u16*)out)[col] = (u16)pk(res, res);
        if (task == 0 && lane == 0) st64(rw, W_EPOCH, tag);
        if (task == 0) {
          const u64 te = rtime();
          u64 mn = ~0ull, mx = 0;
          for (int i = 0; i < 256; i++) { const u64 v = __hip_atomic_load((GAS u64*)(ws + W_STAMP) + i, __ATOMIC_RELAXED, __HIP_MEMORY_SCOPE_AGENT); mn = v < mn ? v : mn; mx = v > mx ? v : mx; }
          const u64 d = AMP == 1 ? te - mn : AMP == 2 ? mx - mn : 0;
          while (rtime() - te < 10000 + d * 100) __builtin_amdgcn_s_sleep(8);
        }
        STAMP(9);
      }
    }
  } else {
    const int r31 = lane & 31, half = lane >> 5;
    if (w < 4) {
      if (lane == 0) __hip_atomic_fetch_add(lp<u32>(L_FLAG + 4 * F_ISS), 1u, __ATOMIC_RELAXED, __HIP_MEMORY_SCOPE_WORKGROUP);
      flag_wait_sleep(F_RDY, 1);
      v4u qd[2];
      for (u32 s = 0; s < SPIN_G; s++) {
        CFENCE();
        qd[0] = ld128(rw, W_B3 + r31 * 1024 + (60 + half) * 16);
        qd[1] = ld128(rw, W_B3 + r31 * 1024 + (62 + half) * 16);
        if (__all(vmax(vmax(0, qd[0]), qd[1]) != SENT)) break;
      }
      VM0();
      STAMP(2);
      const u32 fr = ((r31 & 3) << 2) | ((r31 >> 2) & 3);
      const u32 arow = (32 * w + r31) * 1024;
      v4u a0[16];
#pragma unroll
      for (int s = 0; s < 16; s++) a0[s] = *lp<v4u>(arow + (s >> 3) * 256 + ((((2 * s + half) & 15) ^ fr) << 4));
      BAR();
      STAMP(3);
      f16v sc = {};
      const u32 qrow = LA_Q + r31 * LA_QS + half * 16;
#pragma unroll
      for (int s = 0; s < 32; s++) {
        const v4u av = s < 16 ? a0[s & 15] : *lp<v4u>(arow + (s >> 3) * 256 + ((((2 * s + half) & 15) ^ fr) << 4));
        const v4u bv = s < 30 ? *lp<v4u>(qrow + s * 32) : qd[s & 1];
        sc = mfma32(av, bv, sc);
      }
      float m = fmaxf(fmaxf(fmaxf(sc[0], sc[1]), fmaxf(sc[2], sc[3])), fmaxf(fmaxf(sc[4], sc[5]), fmaxf(sc[6], sc[7])));
      m = fmaxf(m, fmaxf(fmaxf(fmaxf(sc[8], sc[9]), fmaxf(sc[10], sc[11])), fmaxf(fmaxf(sc[12], sc[13]), fmaxf(sc[14], sc[15]))));
      { float a = m, c = m; swap32(a, c); m = fmaxf(a, c); }
      if (lane < 32) *lp<float>(LA_MX + (r31 * 4 + w) * 4) = m;
      BAR();
      STAMP(4);
      { const f4 mm = *lp<f4>(LA_MX + r31 * 16); m = fmaxf(fmaxf(mm.x, mm.y), fmaxf(mm.z, mm.w)); }
      u32 pw[8];
      float l = 0.f;
#pragma unroll
      for (int i = 0; i < 8; i++) {
        pw[i] = pk(__builtin_amdgcn_exp2f(sc[2 * i] - m), __builtin_amdgcn_exp2f(sc[2 * i + 1] - m));
        l = dot2(pw[i], 0x3F803F80u, l);
      }
      *lp<v4u>(LA_P + (2 * w) * 1024 + lane * 16) = v4u{pw[0], pw[1], pw[2], pw[3]};
      *lp<v4u>(LA_P + (2 * w + 1) * 1024 + lane * 16) = v4u{pw[4], pw[5], pw[6], pw[7]};
      l = fold32(l, l);
      if (lane < 32) *lp<float>(LA_LS + (r31 * 4 + w) * 4) = l;
      BAR();
      STAMP(5);
      v4u pf[8];
#pragma unroll
      for (int k = 0; k < 8; k++) pf[k] = *lp<v4u>(LA_P + k * 1024 + lane * 16);
      const u32 blk = (lane >> 4) & 1, q = (lane & 15) >> 2, pp = lane & 3;
      const u32 tb = (4 * half + q) * 1024 + 256 * w + 8 * (pp & 1);
      flag_wait(F_RDYB, 1);
#pragma unroll
      for (int t = 0; t < 4; t++) {
        const u32 ch = 4 * t + 2 * blk + (pp >> 1);
        const u32 ad0 = tb + ((ch ^ ((q << 2) | half)) << 4), ad1 = tb + 8192 + ((ch ^ ((q << 2) | (2 + half))) << 4);
        f16v o = {};
#pragma unroll
        for (int k = 0; k < 8; k++) o = mfma32(trd2(ad0 + k * 16384, ad1 + k * 16384), pf[k], o);
        u32 x0[4], x1[4];
#pragma unroll
        for (int d = 0; d < 4; d++) {
          auto rr = __builtin_amdgcn_permlane16_swap(pk(o[2 * d], o[2 * d + 1]), pk(o[8 + 2 * d], o[9 + 2 * d]), false, false);
          x0[d] = rr[0]; x1[d] = rr[1];
        }
        const u32 so = W_B4 + ((c15 * 16 + 4 * w + t) * 128 + sidx) * 64 + g4 * 16;
        st128(rw, so, v4u{x0[0], x0[1], x0[2], x0[3]});
        st128(rw, so + 16 * 16 * 128 * 64, v4u{x1[0], x1[1], x1[2], x1[3]});
      }
      STAMP(6);
    } else {
      if (w == 8) {
        const v4u sv = {SENT, SENT, SENT, SENT};
#pragma unroll
        for (int i = 0; i < 3; i++) st128(rw, W_SM + sidx * 3072 + i * 1024 + lane * 16, sv);
        VM0();
        if (lane == 0) st64(rw, W_RDYA + sidx * 64, tag);
#pragma nounroll
        for (int i = 0; i < 32; i++) st128(rw, W_B4 + sidx * 32768 + i * 1024 + lane * 16, sv);
        VM0();
        if (lane == 0) st64(rw, W_RDYB + sidx * 64, tag);
        STAMP(2);
        poll_ready(rw, W_RDYA, lane, tag);
        flag_set(F_RDY, 1);
        poll_ready(rw, W_RDYB, lane, tag);
        flag_set(F_RDYB, 1);
      }
      flag_wait_sleep(F_RDY, 1);
      flag_wait_sleep(F_ISS, 4);
      {
        const int h0 = 4 * (w - 4), ch = min(lane, 59);
        v4u a[4];
        for (u32 s = 0; s < SPIN_G; s++) {
          CFENCE();
#pragma unroll
          for (int i = 0; i < 4; i++) a[i] = ld128(rw, W_B3 + (h0 + i) * 1024 + ch * 16);
          u32 m = 0;
#pragma unroll
          for (int i = 0; i < 4; i++) m = vmax(m, a[i]);
          if (__all(m != SENT)) break;
        }
#pragma unroll
        for (int i = 0; i < 4; i++) *lp<v4u>(LA_Q + (h0 + i) * LA_QS + ch * 16) = a[i];
      }
      STAMP(3);
      BAR();
      if (w < 8) {
        const int t = w - 4;
        v4u wr[8];
        const GAS char* wb = (const GAS char*)wuv + (size_t)(128 * cq + 32 * t + 16 * (g4 & 1) + 4 * (g4 >> 1)) * 8192 + hh * 256 + c15 * 16;
#pragma unroll
        for (int i = 0; i < 8; i++) wr[i] = __builtin_nontemporal_load((const GAS v4u*)(wb + ((i & 3) + 8 * (i >> 2)) * 8192));
        BAR();
        BAR();
        v4u wf[8];
#pragma unroll
        for (int d = 0; d < 4; d++) {
          wf[2 * d] = v4u{__builtin_amdgcn_perm(wr[1][d], wr[0][d], 0x05040100u), __builtin_amdgcn_perm(wr[3][d], wr[2][d], 0x05040100u),
                          __builtin_amdgcn_perm(wr[5][d], wr[4][d], 0x05040100u), __builtin_amdgcn_perm(wr[7][d], wr[6][d], 0x05040100u)};
          wf[2 * d + 1] = v4u{__builtin_amdgcn_perm(wr[1][d], wr[0][d], 0x07060302u), __builtin_amdgcn_perm(wr[3][d], wr[2][d], 0x07060302u),
                              __builtin_amdgcn_perm(wr[5][d], wr[4][d], 0x07060302u), __builtin_amdgcn_perm(wr[7][d], wr[6][d], 0x07060302u)};
        }
        asm volatile("" : "+v"(wf[0]), "+v"(wf[1]), "+v"(wf[2]), "+v"(wf[3]), "+v"(wf[4]), "+v"(wf[5]), "+v"(wf[6]), "+v"(wf[7]));
        STAMP(4);
        flag_wait_sleep(F_GO, 1);
        STAMP(5);
        const u32 oo = W_B4 + ((hh * 16 + 4 * cq + t) * 128 + c15) * 64 + g4 * 16;
        float ac[8];
        for (u32 s = 0; s < SPIN_G; s++) {
          CFENCE();
          v4u a[8];
#pragma unroll
          for (int i = 0; i < 8; i++) a[i] = ld128(rw, oo + i * 1024);
          flag_wait(F_WR, 1);
          const v4u w0 = *lp<v4u>(LA_W + c15 * 32), w1 = *lp<v4u>(LA_W + c15 * 32 + 16);
          u32 m = 0;
#pragma unroll
          for (int e = 0; e < 8; e++) ac[e] = 0.f;
#pragma unroll
          for (int i = 0; i < 8; i++) {
            const u32 wl = i < 4 ? w0[i & 3] : w1[i & 3], wh = wl << 16;
            m = vmax(m, a[i]);
#pragma unroll
            for (int d = 0; d < 4; d++) { ac[2 * d] = dot2(a[i][d], wl, ac[2 * d]); ac[2 * d + 1] = dot2(a[i][d], wh, ac[2 * d + 1]); }
          }
          if (__all(m != SENT)) break;
        }
        STAMP(6);
        const v4u af = {pk(ac[0], ac[1]), pk(ac[2], ac[3]), pk(ac[4], ac[5]), pk(ac[6], ac[7])};
        float t8[8];
#pragma unroll
        for (int i = 0; i < 8; i++) { const f4 d = mfma16(af, wf[i], f4{0.f, 0.f, 0.f, 0.f}); t8[i] = (d.x + d.y) + (d.z + d.w); }
        const float u0 = fold16(fold32(t8[0], t8[4]), fold32(t8[2], t8[6])), u1 = fold16(fold32(t8[1], t8[5]), fold32(t8[3], t8[7]));
        *lp<f2>(LA_RED + t * 512 + lane * 8) = f2{u0, u1};
        u32 old = 0;
        if (lane == 0) old = __hip_atomic_fetch_add(lp<u32>(L_FLAG + 4 * F_CNT), 1u, __ATOMIC_RELAXED, __HIP_MEMORY_SCOPE_WORKGROUP);
        if (__builtin_amdgcn_readfirstlane(old) == 3) {
          CFENCE();
          const f2 r0 = *lp<f2>(LA_RED + lane * 8), r1 = *lp<f2>(LA_RED + 512 + lane * 8), r2 = *lp<f2>(LA_RED + 1024 + lane * 8), r3 = *lp<f2>(LA_RED + 1536 + lane * 8);
          const float il = __uint_as_float(flag_get(F_INVL));
          const f2 rs = (r0 + r1) + (r2 + r3);
          st32(rw, W_B5 + (hh * 4 + cq) * 256 + c15 * 16 + g4 * 4, pk(rs.x * il, rs.y * il));
        }
        STAMP(7);
      } else {
        BAR();
        BAR();
        if (w == 9) {
          const f4 mm = *lp<f4>(LA_MX + r31 * 16), ll = *lp<f4>(LA_LS + r31 * 16);
          const float m = fmaxf(fmaxf(mm.x, mm.y), fmaxf(mm.z, mm.w)), l = (ll.x + ll.y) + (ll.z + ll.w);
          if (lane < 32) st64(rw, W_ML + sidx * 256 + lane * 8, v2u{__float_as_uint(m), __float_as_uint(l)});
          STAMP(4);
        } else if (w == 8) {
          v2u a, c;
          for (u32 s = 0; s < SPIN_G; s++) {
            CFENCE();
            a = ld64(rw, W_ML + lane * 256 + hh * 8); c = ld64(rw, W_ML + (lane + 64) * 256 + hh * 8);
            if (__all(umax3(max(a.x, a.y), c.x, c.y) != SENT)) break;
            __builtin_amdgcn_s_sleep(1);
          }
          flag_set(F_GO, 1);
          STAMP(5);
          const float M = wmax(fmaxf(__uint_as_float(a.x), __uint_as_float(c.x)));
          const u32 e = pk(__builtin_amdgcn_exp2f(__uint_as_float(a.x) - M), __builtin_amdgcn_exp2f(__uint_as_float(c.x) - M));
          *lp<u32>(LA_W + c15 * 32 + g4 * 4) = e & 0xFFFFu;
          *lp<u32>(LA_W + c15 * 32 + 16 + g4 * 4) = e >> 16;
          const float L = wsum(bflo(e) * __uint_as_float(a.y) + bfhi(e) * __uint_as_float(c.y));
          flag_set(F_INVL, __float_as_uint(__builtin_amdgcn_rcpf(L)));
          flag_set(F_WR, 1);
          STAMP(6);
        }
      }
    }
  }
  STAMP_OUT();
}

extern "C" void kernel_launch(void* const* d_in, const int* in_sizes, int n_in, void* d_out, int out_size, void* d_ws, size_t ws_size,
                              hipStream_t stream) {
  fused_decode<<<256, 768, 0, stream>>>((const u16*)d_in[0], (const u16*)d_in[1], (const u16*)d_in[2], (const u16*)d_in[3], (const u16*)d_in[4],
                                        (const u16*)d_in[5], (const u16*)d_in[7], (const u16*)d_in[8], (const u16*)d_in[9], (const u16*)d_in[10],
                                        (const u16*)d_in[11], (u16*)d_out, (char*)d_ws);
}
```

```cpp
#include <hip/hip_runtime.h>
#include <hip/hip_bf16.h>
typedef unsigned short u16;
__device__ __forceinline__ float bf2f(u16 b){ return __uint_as_float(((unsigned)b)<<16); }
__device__ __forceinline__ u16 f2bf(float f){ unsigned u=__float_as_uint(f); u += 0x7fffu + ((u>>16)&1u); return (u16)(u>>16); }
__device__ __forceinline__ float rbf(float f){ return bf2f(f2bf(f)); }
constexpr int M=576,H=32,D=128,DC=512,SK=16384,F=4608;

__global__ void k_mod(const u16* c,const u16* wmod,float* mod){
  __shared__ float s[M];
  for(int i=threadIdx.x;i<M;i+=blockDim.x){ float v=bf2f(c[i]); s[i]=rbf(v/(1.f+__expf(-v))); }
  __syncthreads();
  int n=blockIdx.x*blockDim.x+threadIdx.x;
  if(n<6*M){ float a=0; for(int k=0;k<M;k++) a+=s[k]*bf2f(wmod[k*6*M+n]); mod[n]=rbf(a);} }

__device__ void ln_mod(const float* xs,float* hs,const float* sh,const float* sc){
  float m=0; for(int i=0;i<M;i++) m+=xs[i]; m/=M;
  float v=0; for(int i=0;i<M;i++){ float d=xs[i]-m; v+=d*d;} v/=M;
  float r=1.f/sqrtf(v+1e-5f);
  for(int i=threadIdx.x;i<M;i+=blockDim.x) hs[i]=rbf(((xs[i]-m)*r)*(1.f+sc[i])+sh[i]);
  __syncthreads();
}
__global__ void k_q(const u16* x,const float* mod,const u16* wq,float* q){
  __shared__ float xs[M],hs[M];
  for(int i=threadIdx.x;i<M;i+=blockDim.x) xs[i]=bf2f(x[i]);
  __syncthreads();
  ln_mod(xs,hs,mod,mod+M);
  int n=blockIdx.x*blockDim.x+threadIdx.x;
  float a=0; for(int k=0;k<M;k++) a+=hs[k]*bf2f(wq[k*H*D+n]); q[n]=rbf(a);
}
__global__ void k_qabs(const float* q,const u16* wuk,float* qa){
  int i=blockIdx.x*blockDim.x+threadIdx.x; int h=i/DC,c=i%DC;
  float a=0; for(int d=0;d<D;d++) a+=q[h*D+d]*bf2f(wuk[(c*H+h)*D+d]); qa[i]=a*0.08838834764831845f;
}
__global__ void k_scores(const float* qa,const u16* ckv,float* sc){
  __shared__ float qs[H*DC/2];
  int k=blockIdx.x*64+(threadIdx.x&63); int hg=threadIdx.x>>6;
  for(int half=0;half<2;half++){
    __syncthreads();
    for(int i=threadIdx.x;i<H*DC/2;i+=256) qs[i]=qa[half*H*DC/2+i];
    __syncthreads();
    float a[4]={0,0,0,0};
    for(int c=0;c<DC;c++){ float v=bf2f(ckv[(size_t)k*DC+c]); for(int j=0;j<4;j++) a[j]+=v*qs[(hg*4+j)*DC+c]; }
    for(int j=0;j<4;j++) sc[(half*16+hg*4+j)*SK+k]=a[j];
  }
}
__global__ void k_soft(float* sc){
  __shared__ float red[256];
  float* s=sc+blockIdx.x*SK; float m=-1e30f;
  for(int i=threadIdx.x;i<SK;i+=256) m=fmaxf(m,s[i]);
  red[threadIdx.x]=m; __syncthreads();
  for(int o=128;o;o>>=1){ if(threadIdx.x<o) red[threadIdx.x]=fmaxf(red[threadIdx.x],red[threadIdx.x+o]); __syncthreads(); }
  m=red[0]; __syncthreads();
  float l=0; for(int i=threadIdx.x;i<SK;i+=256){ float e=__expf(s[i]-m); s[i]=e; l+=e; }
  red[threadIdx.x]=l; __syncthreads();
  for(int o=128;o;o>>=1){ if(threadIdx.x<o) red[threadIdx.x]+=red[threadIdx.x+o]; __syncthreads(); }
  l=1.f/red[0];
  for(int i=threadIdx.x;i<SK;i+=256) s[i]*=l;
}
__global__ void k_pv(const float* sc,const u16* ckv,float* part){
  int c=threadIdx.x; int k0=blockIdx.x*128;
  __shared__ float ps[H*128];
  for(int i=threadIdx.x;i<H*128;i+=512) ps[i]=sc[(i/128)*SK+k0+(i%128)];
  __syncthreads();
  float a[H]; for(int h=0;h<H;h++) a[h]=0;
  for(int k=0;k<128;k++){ float v=bf2f(ckv[(size_t)(k0+k)*DC+c]);
    #pragma unroll
    for(int h=0;h<H;h++) a[h]+=v*ps[h*128+k]; }
  for(int h=0;h<H;h++) part[((size_t)blockIdx.x*H+h)*DC+c]=a[h];
}
__global__ void k_ctx(const float* part,const u16* wuv,float* ctx){
  int h=blockIdx.x; __shared__ float cs[DC];
  int c=threadIdx.x; float a=0; for(int s=0;s<128;s++) a+=part[((size_t)s*H+h)*DC+c]; cs[c]=a; __syncthreads();
  if(threadIdx.x<D){ float o=0; for(int cc=0;cc<DC;cc++) o+=cs[cc]*bf2f(wuv[(cc*H+h)*D+threadIdx.x]); ctx[h*D+threadIdx.x]=rbf(o);} }
__global__ void k_o(const float* ctx,const u16* wo,const u16* x,const float* mod,float* x2){
  int m=blockIdx.x*64+(threadIdx.x&63); int g=threadIdx.x>>6; __shared__ float red[16][64];
  float a=0; for(int k=g*256;k<g*256+256;k++) a+=ctx[k]*bf2f(wo[k*M+m]); red[g][threadIdx.x&63]=a; __syncthreads();
  if(g==0){ float t=0; for(int j=0;j<16;j++) t+=red[j][threadIdx.x]; x2[m]=rbf(bf2f(x[m])+rbf(mod[2*M+m]*rbf(t))); } }
__global__ void k_gu(const float* x2,const float* mod,const u16* wg,const u16* wu,float* f){
  __shared__ float xs[M],hs[M];
  for(int i=threadIdx.x;i<M;i+=blockDim.x) xs[i]=x2[i];
  __syncthreads();
  ln_mod(xs,hs,mod+3*M,mod+4*M);
  int n=blockIdx.x*blockDim.x+threadIdx.x;
  float g=0,u=0; for(int k=0;k<M;k++){ g+=hs[k]*bf2f(wg[k*F+n]); u+=hs[k]*bf2f(wu[k*F+n]); }
  g=rbf(g); u=rbf(u);
  float t=tanhf(0.7978845608028654f*(g+0.044715f*g*g*g)); f[n]=rbf(rbf(0.5f*g*(1.f+t))*u);
}
__global__ void k_down(const float* f,const u16* wd,const float* x2,const float* mod,u16* out){
  int m=blockIdx.x*64+(threadIdx.x&63); int g=threadIdx.x>>6; __shared__ float red[16][64];
  float a=0; for(int k=g*288;k<g*288+288;k++) a+=f[k]*bf2f(wd[k*M+m]); red[g][threadIdx.x&63]=a; __syncthreads();
  if(g==0){ float t=0; for(int j=0;j<16;j++) t+=red[j][threadIdx.x]; out[m]=f2bf(x2[m]+rbf(mod[5*M+m]*rbf(t))); } }


typedef unsigned long long u64;
#define GAS __attribute__((address_space(1)))
template<int T,int LDS> __global__ void __launch_bounds__(T) k_first(u64* st){
  __shared__ char smem[LDS];
  asm volatile(""::"v"((__attribute__((address_space(3))) char*)smem));
  u64 t0=__builtin_amdgcn_s_memrealtime();
  if(threadIdx.x==0) st[blockIdx.x]=t0;
}
template<int T,int LDS> __global__ void __launch_bounds__(T) k_last(const u64* st,int n){
  __shared__ char smem[LDS];
  asm volatile(""::"v"((__attribute__((address_space(3))) char*)smem));
  u64 mn=~0ull; for(int i=0;i<n;i++){ u64 v=st[i]; mn=v<mn?v:mn; }
  while(__builtin_amdgcn_s_memrealtime()-mn < 50000) __builtin_amdgcn_s_sleep(1);
}

extern "C" void kernel_launch(void* const* d_in, const int* in_sizes, int n_in, void* d_out, int out_size, void* d_ws, size_t ws_size, hipStream_t stream){
  const u16 *x=(const u16*)d_in[0],*ckv=(const u16*)d_in[1],*wq=(const u16*)d_in[2],*wuk=(const u16*)d_in[3],*wuv=(const u16*)d_in[4],*wo=(const u16*)d_in[5];
  const u16 *wg=(const u16*)d_in[7],*wu=(const u16*)d_in[8],*wd=(const u16*)d_in[9],*c=(const u16*)d_in[10],*wmod=(const u16*)d_in[11];
  float* ws=(float*)d_ws; float* mod=ws; float* q=mod+4096; float* qa=q+4096; float* ctx=qa+H*DC; float* x2=ctx+4096; float* f=x2+1024; float* sc=f+8192; float* part=sc+H*SK;
  u64* stp=(u64*)((char*)d_ws+(200u<<20));
  k_first<64,16><<<1,64,0,stream>>>(stp);
  k_mod<<<54,64,0,stream>>>(c,wmod,mod);
  k_q<<<64,64,0,stream>>>(x,mod,wq,q);
  k_qabs<<<64,256,0,stream>>>(q,wuk,qa);
  k_scores<<<256,256,0,stream>>>(qa,ckv,sc);
  k_soft<<<32,256,0,stream>>>(sc);
  k_pv<<<128,512,0,stream>>>(sc,ckv,part);
  k_ctx<<<32,512,0,stream>>>(part,wuv,ctx);
  k_o<<<9,1024,0,stream>>>(ctx,wo,x,mod,x2);
  k_gu<<<72,64,0,stream>>>(x2,mod,wg,wu,f);
  k_down<<<9,1024,0,stream>>>(f,wd,x2,mod,(u16*)d_out);
  k_last<64,16><<<1,64,0,stream>>>(stp,1);
}
```

```cpp
#include <hip/hip_runtime.h>
#include <stdint.h>
typedef unsigned u32;
extern "C" __global__ void __attribute__((aligned(4096))) __launch_bounds__(768, 3)
fused_decode(const void* ckv, const void* wmod, const void* wq, const void* wuk, const void* x, const void* cvec, void* ws,
             const void* wuv, const void* wo, const void* wg, const void* wu, const void* wd, void* out) {
#if defined(__HIP_DEVICE_COMPILE__)
  __shared__ __attribute__((aligned(16))) char smem[163840];
  asm volatile(
    ".Lfd_start:\n"
    "s_load_dwordx16 s[4:19], s[0:1], 0x0\n"
    "v_readfirstlane_b32 s3, v0\n"
    "s_memrealtime s[94:95]\n"
    "v_and_b32 v1, 63, v0\n"
    "s_lshr_b32 s21, s2, 3\n"
    "s_lshr_b32 s3, s3, 6\n"
    "s_cmp_eq_u32 s3, 11\n"
    "s_cbranch_scc1 .Lfd_warm\n"
    ".Lfd_go:\n"
    "s_mov_b64 s[96:97], s[0:1]\n"
    "s_mov_b32 s98, s2\n"
    "v_mov_b32 v160, v0\n"
    "s_and_b32 s20, s2, 7\n"
    "s_cmp_lt_u32 s21, 16\n"
    "s_cbranch_scc1 .Lfd_q\n"
    "s_cmp_lt_u32 s3, 9\n"
    "s_cbranch_scc1 .Lfd_ao\n"
    "s_sub_u32 s21, s21, 16\n"
    "s_add_u32 s23, s20, 10\n"
    "s_cmp_lt_u32 s20, 6\n"
    "s_cselect_b32 s22, s20, s21\n"
    "s_cselect_b32 s23, s21, s23\n"
    "s_cmp_ge_u32 s22, 6\n"
    "s_cbranch_scc1 .Lfd_ao\n"
    "s_cmp_lg_u32 s3, 9\n"
    "s_cbranch_scc1 .Lfd_ah\n"
    "s_mul_i32 s22, s22, 0xa2000\n"
    "s_lshl_b32 s23, s23, 7\n"
    "v_lshrrev_b32 v6, 3, v1\n"
    "v_lshlrev_b32 v7, 4, v1\n"
    "v_lshlrev_b32 v8, 1, v1\n"
    "s_add_u32 s22, s22, s23\n"
    "v_and_b32 v7, 0x70, v7\n"
    "v_and_b32 v8, 32, v8\n"
    "v_mul_u32_u24 v6, 0x1b00, v6\n"
    "v_xor_b32 v7, v7, v8\n"
    "s_mov_b32 m0, 0x20000\n"
    "v_add3_u32 v2, v6, v7, s22\n"
    "v_xor_b32 v6, 64, v2\n"
    "v_add_u32 v4, 0x1a800, v2\n"
    "v_add_u32 v3, 0xd400, v6\n"
    "v_add_u32 v5, 0x27c00, v6\n"
    "s_waitcnt lgkmcnt(0)\n"
    "global_load_lds_dwordx4 v2, s[6:7] nt\n"
    "global_load_lds_dwordx4 v3, s[6:7] offset:1024 nt\n"
    "global_load_lds_dwordx4 v4, s[6:7] offset:2048 nt\n"
    "global_load_lds_dwordx4 v5, s[6:7] offset:3072 nt\n"
    "s_add_u32 s6, s6, 0x36000\n"
    "s_addc_u32 s7, s7, 0\n"
    "s_add_u32 m0, m0, 0x1000\n"
    "s_nop 0\n"
    "global_load_lds_dwordx4 v2, s[6:7] nt\n"
    "global_load_lds_dwordx4 v3, s[6:7] offset:1024 nt\n"
    "global_load_lds_dwordx4 v4, s[6:7] offset:2048 nt\n"
    "global_load_lds_dwordx4 v5, s[6:7] offset:3072 nt\n"
    "s_add_u32 s6, s6, 0x36000\n"
    "s_addc_u32 s7, s7, 0\n"
    "s_add_u32 m0, m0, 0x1000\n"
    "s_nop 0\n"
    "global_load_lds_dwordx4 v2, s[6:7] nt\n"
    "global_load_lds_dwordx4 v3, s[6:7] offset:1024 nt\n"
    "global_load_lds_dwordx4 v4, s[6:7] offset:2048 nt\n"
    "global_load_lds_dwordx4 v5, s[6:7] offset:3072 nt\n"
    "s_barrier\n"
    "s_branch .Lfd_R_S1\n"
    ".Lfd_ah:\n"
    "s_cmp_eq_u32 s3, 11\n"
    "s_cbranch_scc1 .Lfd_asi\n"
    "s_sub_u32 s24, s23, 9\n"
    "s_cmp_ge_u32 s23, 9\n"
    "s_cselect_b32 s23, s24, s23\n"
    "s_lshl_b32 s23, s23, 7\n"
    "v_lshlrev_b32 v2, 3, v1\n"
    "v_lshl_add_u32 v3, v1, 1, s23\n"
    "v_mov_b32 v166, 0\n"
    "v_mov_b32 v167, 0\n"
    "s_waitcnt lgkmcnt(0)\n"
    "global_load_dwordx2 v[162:163], v2, s[12:13]\n"
    "global_load_dwordx2 v[164:165], v2, s[12:13] offset:512\n"
    "global_load_ushort v161, v3, s[12:13]\n"
    "s_mov_b64 exec, 0xffff\n"
    "global_load_dwordx2 v[166:167], v2, s[12:13] offset:1024\n"
    "s_mov_b64 exec, -1\n"
    "s_barrier\n"
    "s_branch .Lfd_R_LN\n"
    ".Lfd_asi:\n"
    "s_mulk_i32 s22, 0xc0\n"
    "v_min_u32 v2, 47, v1\n"
    "v_mov_b32 v3, 0\n"
    "v_lshl_add_u32 v2, v2, 2, s22\n"
    "s_waitcnt lgkmcnt(0)\n"
    "global_load_dword v161, v2, s[14:15]\n"
    "global_load_dword v162, v3, s[16:17] sc1\n"
    "s_barrier\n"
    "s_branch .Lfd_R_SI\n"
    ".Lfd_q:\n"
    "s_mul_i32 s24, s3, 0x3000\n"
    "s_cmp_lt_u32 s21, 9\n"
    "s_cbranch_scc1 .Lfd_q3\n"
    "s_mul_i32 s23, s21, 7\n"
    "s_add_u32 s22, s3, s23\n"
    "s_sub_u32 s22, s22, 63\n"
    "s_cmp_ge_u32 s3, 7\n"
    "s_cbranch_scc1 .Lfd_qo\n"
    "s_cmp_ge_u32 s22, 48\n"
    "s_cbranch_scc1 .Lfd_qidle\n"
    "s_and_b32 s23, s22, 7\n"
    "s_lshr_b32 s22, s22, 3\n"
    "s_mul_i32 s22, s22, 0xc0000\n"
    "s_lshl3_add_u32 s23, s20, s23\n"
    "s_lshl_b32 s23, s23, 7\n"
    "v_lshlrev_b32 v6, 10, v1\n"
    "v_lshlrev_b32 v7, 4, v1\n"
    "v_lshlrev_b32 v8, 1, v1\n"
    "s_add_u32 s22, s22, s23\n"
    "v_and_b32 v6, 0xe000, v6\n"
    "v_and_b32 v7, 0x70, v7\n"
    "v_and_b32 v8, 32, v8\n"
    "v_xor_b32 v7, v7, v8\n"
    "s_mov_b32 m0, s24\n"
    "v_add3_u32 v2, v6, v7, s22\n"
    "v_xor_b32 v6, 64, v2\n"
    "v_add_u32 v4, 0x1f800, v2\n"
    "v_add_u32 v3, 0xfc00, v6\n"
    "v_add_u32 v5, 0x2f400, v6\n"
    "s_waitcnt lgkmcnt(0)\n"
    "global_load_lds_dwordx4 v2, s[8:9] nt\n"
    "global_load_lds_dwordx4 v3, s[8:9] offset:1024 nt\n"
    "global_load_lds_dwordx4 v4, s[8:9] offset:2048 nt\n"
    "global_load_lds_dwordx4 v5, s[8:9] offset:3072 nt\n"
    "s_add_u32 s8, s8, 0x40000\n"
    "s_addc_u32 s9, s9, 0\n"
    "s_add_u32 m0, m0, 0x1000\n"
    "s_nop 0\n"
    "global_load_lds_dwordx4 v2, s[8:9] nt\n"
    "global_load_lds_dwordx4 v3, s[8:9] offset:1024 nt\n"
    "global_load_lds_dwordx4 v4, s[8:9] offset:2048 nt\n"
    "global_load_lds_dwordx4 v5, s[8:9] offset:3072 nt\n"
    "s_add_u32 s8, s8, 0x40000\n"
    "s_addc_u32 s9, s9, 0\n"
    "s_add_u32 m0, m0, 0x1000\n"
    "s_nop 0\n"
    "global_load_lds_dwordx4 v2, s[8:9] nt\n"
    "global_load_lds_dwordx4 v3, s[8:9] offset:1024 nt\n"
    "global_load_lds_dwordx4 v4, s[8:9] offset:2048 nt\n"
    "global_load_lds_dwordx4 v5, s[8:9] offset:3072 nt\n"
    "s_branch .Lfd_R_S2\n"
    ".Lfd_q3:\n"
    "s_cmp_ge_u32 s3, 8\n"
    "s_cbranch_scc1 .Lfd_qo\n"
    "s_waitcnt lgkmcnt(0)\n"
    "s_cmp_lt_u32 s3, 6\n"
    "s_cbranch_scc1 .Lfd_R_FG\n"
    "s_branch .Lfd_R_FU\n"
    ".Lfd_warm:\n"
    "s_getpc_b64 s[24:25]\n"
    ".Lfd_pc:\n"
    "s_lshl_b32 s22, s21, 10\n"
    "v_lshl_add_u32 v2, v1, 4, s22\n"
    "v_min_u32 v2, .Lfd_end-.Lfd_pc-16, v2\n"
    "global_load_dwordx4 v[164:167], v2, s[24:25]\n"
    "v_lshlrev_b32 v2, 2, v1\n"
    "v_mov_b32 v3, 0\n"
    "v_or_b32 v2, 0x27fc0, v2\n"
    "ds_write_b32 v2, v3\n"
    "s_branch .Lfd_go\n"
    ".Lfd_qo:\n"
    "v_mov_b32 v3, 0\n"
    "s_waitcnt lgkmcnt(0)\n"
    "global_load_dwordx2 v[162:163], v3, s[16:17] sc1\n"
    "s_branch .Lfd_qo2\n"
    ".Lfd_ao:\n"
    "s_cmp_lt_u32 s3, 4\n"
    "s_cbranch_scc1 .Lfd_apf\n"
    "s_waitcnt lgkmcnt(0)\n"
    "s_barrier\n"
    "s_cmp_eq_u32 s3, 8\n"
    "s_cbranch_scc1 .Lfd_R_AH8\n"
    "s_cmp_lt_u32 s3, 8\n"
    "s_cbranch_scc1 .Lfd_R_AS\n"
    "s_branch .Lfd_R_AH\n"
    ".Lfd_apf:\n"
    "s_waitcnt lgkmcnt(0)\n"
    "s_barrier\n"
    "s_branch .Lfd_R_AM\n"
    ".Lfd_qo2:\n"
    "s_cmp_lt_u32 s21, 9\n"
    "s_cbranch_scc1 .Lfd_R_FV\n"
    "s_cmp_lt_u32 s3, 9\n"
    "s_cbranch_scc1 .Lfd_R_HG\n"
    "s_cmp_eq_u32 s3, 9\n"
    "s_cbranch_scc1 .Lfd_R_QT\n"
    ".Lfd_qidle:\n"
    "s_waitcnt lgkmcnt(0)\n"
    "s_barrier\n"
    ".Lfd_pf2:\n"
    "s_mov_b64 s[0:1], s[96:97]\n"
    "s_mov_b32 s2, s98\n"
    "v_mov_b32 v0, v160\n"
    "s_barrier\n"
    "s_branch .Lfd_R_PF\n"
    ".Lfd_as2:\n"
    "s_mov_b64 s[0:1], s[96:97]\n"
    "s_mov_b32 s2, s98\n"
    "v_mov_b32 v0, v160\n"
    "s_branch .Lfd_R_AH\n"
    ".Lfd_R_AH8:\n"
    "s_bfe_u32 s3, s2, 0x30003\n"
    "s_add_i32 s5, s3, -3\n"
    "s_add_i32 s6, s2, 0xffffff40\n"
    "s_add_i32 s4, s3, -6\n"
    "s_min_u32 s5, s5, s3\n"
    "s_cmp_gt_u32 s3, 5\n"
    "s_cselect_b32 s4, s4, s5\n"
    "s_cmp_eq_u32 s4, 0\n"
    "v_readfirstlane_b32 s3, v0\n"
    "s_cbranch_scc1 .Lfd_R_AH8_22\n"
    "s_lshl_b32 s4, s4, 21\n"
    "s_lshl_b32 s5, s6, 14\n"
    "s_add_i32 s13, s4, s5\n"
    "s_lshl_b32 s4, s6, 13\n"
    "s_cbranch_execnz .Lfd_R_AH8_3\n"
    ".Lfd_R_AH8_2:\n"
    "s_lshl_b32 s7, s2, 7\n"
    "s_and_b32 s5, s4, 0xffffc000\n"
    "s_and_b32 s7, s7, 0x80\n"
    "s_or_b32 s5, s5, s7\n"
    "s_add_i32 s13, s5, 0x10000\n"
    ".Lfd_R_AH8_3:\n"
    "s_load_dwordx2 s[8:9], s[0:1], 0x30\n"
    "s_and_b32 s12, s2, 7\n"
    "s_add_i32 s0, s2, 0xffffff80\n"
    "s_add_i32 s20, s4, 0x10000\n"
    "v_and_b32_e32 v6, 63, v0\n"
    "s_waitcnt lgkmcnt(0)\n"
    "s_and_b32 s9, s9, 0xffff\n"
    "s_cmp_lt_u32 s0, 64\n"
    "s_cselect_b64 s[4:5], -1, 0\n"
    "s_and_b64 s[0:1], s[4:5], exec\n"
    "s_cselect_b32 s21, 0x7ff00000, 0\n"
    "s_cmp_lt_u32 s12, 6\n"
    "s_cselect_b64 s[14:15], -1, 0\n"
    "v_cmp_lt_u32_e64 s[0:1], 7, v6\n"
    "s_or_b64 s[14:15], s[14:15], s[4:5]\n"
    "s_mov_b32 s16, -1\n"
    "v_lshlrev_b32_e32 v1, 4, v6\n"
    "v_mov_b32_e32 v5, 0x7ff00000\n"
    "s_or_b64 vcc, s[14:15], s[0:1]\n"
    "s_mov_b32 s17, s16\n"
    "v_add_u32_e32 v2, s13, v1\n"
    "v_cndmask_b32_e32 v3, 0, v5, vcc\n"
    "s_mov_b32 s18, s16\n"
    "s_mov_b32 s19, s16\n"
    "v_mov_b64_e32 v[8:9], s[16:17]\n"
    "s_or_b32 s4, s21, s20\n"
    "s_mov_b32 s11, 0x20000\n"
    "s_mov_b32 s10, 0x600000\n"
    "v_or_b32_e32 v4, v2, v3\n"
    "v_mov_b64_e32 v[10:11], s[18:19]\n"
    "v_or_b32_e32 v2, s4, v1\n"
    "s_movk_i32 s4, 0x1000\n"
    "buffer_store_dwordx4 v[8:11], v4, s[8:11], 0 offen sc1\n"
    "buffer_store_dwordx4 v[8:11], v2, s[8:11], 0 offen sc1\n"
    "buffer_store_dwordx4 v[8:11], v2, s[8:11], 0 offen offset:1024 sc1\n"
    "buffer_store_dwordx4 v[8:11], v2, s[8:11], 0 offen offset:2048 sc1\n"
    "buffer_store_dwordx4 v[8:11], v2, s[8:11], 0 offen offset:3072 sc1\n"
    "buffer_store_dwordx4 v[8:11], v2, s[8:11], s4 offen sc1\n"
    "buffer_store_dwordx4 v[8:11], v2, s[8:11], s4 offen offset:1024 sc1\n"
    "buffer_store_dwordx4 v[8:11], v2, s[8:11], s4 offen offset:2048 sc1\n"
    "buffer_store_dwordx4 v[8:11], v2, s[8:11], s4 offen offset:3072 sc1\n"
    "s_lshl_b32 s4, s6, 7\n"
    "s_addk_i32 s4, 0x2000\n"
    "buffer_load_dwordx2 v[2:3], off, s[8:11], 0 sc1\n"
    "v_lshl_add_u32 v7, v6, 3, s4\n"
    "v_mov_b32_e32 v8, s21\n"
    "v_cmp_lt_u32_e64 s[4:5], 15, v6\n"
    "s_mov_b32 s7, 0\n"
    "s_movk_i32 s6, 0x2000\n"
    "v_cndmask_b32_e64 v5, v8, v5, s[4:5]\n"
    "v_or_b32_e32 v5, v7, v5\n"
    "s_and_b64 vcc, exec, s[14:15]\n"
    "s_cbranch_vccnz .Lfd_R_AH8_5\n"
    "v_mov_b32_e32 v8, -1\n"
    "v_mov_b32_e32 v9, v8\n"
    "v_mov_b32_e32 v10, v8\n"
    "v_mov_b32_e32 v11, v8\n"
    "s_waitcnt vmcnt(0)\n"
    "buffer_store_dwordx4 v[8:11], v4, s[8:11], 0 offen sc1\n"
    "buffer_store_dwordx4 v[8:11], v4, s[8:11], s6 offen sc1\n"
    "s_waitcnt vmcnt(1)\n"
    "s_memtime s[14:15]\n"
    "s_waitcnt vmcnt(0)\n"
    "s_memtime s[16:17]\n"
    "s_waitcnt lgkmcnt(0)\n"
    "s_sub_i32 s6, s16, s14\n"
    "s_cmpk_gt_u32 s6, 0x78\n"
    "s_cselect_b32 s7, 0x80000000, 0\n"
    ".Lfd_R_AH8_5:\n"
    "s_waitcnt vmcnt(0)\n"
    "s_brev_b32 s13, -2\n"
    "v_lshl_add_u64 v[2:3], v[2:3], 0, 1\n"
    "v_and_b32_e32 v4, 0x7fffffff, v3\n"
    "v_mov_b32_e32 v8, v2\n"
    "v_or_b32_e32 v9, s7, v4\n"
    "buffer_store_dwordx2 v[8:9], v5, s[8:11], 0 offen sc1\n"
    "s_barrier\n"
    "s_waitcnt lgkmcnt(0)\n"
    "s_mov_b32 s6, s94\n"
    "s_memrealtime s[14:15]\n"
    "s_waitcnt lgkmcnt(0)\n"
    "s_sub_i32 s7, s14, s6\n"
    "s_cmpk_gt_u32 s7, 0x103\n"
    "s_cbranch_scc1 .Lfd_R_AH8_7\n"
    ".Lfd_R_AH8_6:\n"
    "s_sleep 2\n"
    "s_memrealtime s[14:15]\n"
    "s_waitcnt lgkmcnt(0)\n"
    "s_sub_i32 s7, s14, s6\n"
    "s_cmpk_lt_u32 s7, 0x104\n"
    "s_cbranch_scc1 .Lfd_R_AH8_6\n"
    ".Lfd_R_AH8_7:\n"
    "v_lshlrev_b32_e32 v8, 7, v6\n"
    "v_or_b32_e32 v9, 0x2000, v8\n"
    "s_mov_b32 s14, 0x8000\n"
    "s_branch .Lfd_R_AH8_9\n"
    ".Lfd_R_AH8_8:\n"
    "s_andn2_b64 vcc, exec, s[6:7]\n"
    "s_cbranch_vccz .Lfd_R_AH8_11\n"
    ".Lfd_R_AH8_9:\n"
    "buffer_load_dwordx2 v[4:5], v9, s[8:11], 0 offen sc1\n"
    "s_waitcnt vmcnt(0)\n"
    "v_cmp_eq_u32_e32 vcc, v4, v2\n"
    "v_bitop3_b32 v4, v5, s13, v3 bitop3:0x48\n"
    "v_cmp_eq_u32_e64 s[6:7], 0, v4\n"
    "s_and_b64 s[6:7], vcc, s[6:7]\n"
    "s_nop 0\n"
    "v_cndmask_b32_e64 v4, 0, 1, s[6:7]\n"
    "v_cmp_ne_u32_e32 vcc, 0, v4\n"
    "s_cmp_eq_u64 vcc, exec\n"
    "s_mov_b64 s[6:7], -1\n"
    "s_cbranch_scc1 .Lfd_R_AH8_8\n"
    "s_add_i32 s14, s14, -1\n"
    "s_cmp_eq_u32 s14, 0\n"
    "s_cselect_b64 s[6:7], -1, 0\n"
    "s_sleep 1\n"
    "s_branch .Lfd_R_AH8_8\n"
    ".Lfd_R_AH8_11:\n"
    "v_cmp_gt_i32_e32 vcc, 0, v5\n"
    "s_and_b32 s7, vcc_hi, 0xc00000\n"
    "s_and_b32 s6, vcc_lo, 0xc00000c0\n"
    "s_lshl_b32 s14, s12, 2\n"
    "s_mov_b32 s13, 0\n"
    "s_bcnt1_i32_b64 s12, s[6:7]\n"
    "v_cmp_gt_u64_e64 s[6:7], s[12:13], 3\n"
    "s_bfe_u32 s15, s2, 0x20005\n"
    "v_lshlrev_b32_e32 v20, 4, v0\n"
    "v_cndmask_b32_e64 v3, 0, 1, s[6:7]\n"
    "s_and_b32 s7, vcc_hi, 0xc00000c0\n"
    "s_and_b32 s6, vcc_lo, 0xc000\n"
    "s_bcnt1_i32_b64 s12, s[6:7]\n"
    "v_cmp_gt_u64_e64 s[6:7], s[12:13], 3\n"
    "s_and_b64 s[6:7], s[6:7], exec\n"
    "s_cselect_b32 s6, 2, 0\n"
    "v_or_b32_e32 v3, s6, v3\n"
    "s_and_b32 s7, vcc_hi, 0xc000\n"
    "s_and_b32 s6, vcc_lo, 0xc00000\n"
    "s_bcnt1_i32_b64 s12, s[6:7]\n"
    "v_cmp_gt_u64_e64 s[6:7], s[12:13], 2\n"
    "s_and_b64 s[6:7], s[6:7], exec\n"
    "s_cselect_b32 s6, 4, 0\n"
    "v_or_b32_e32 v3, s6, v3\n"
    "v_lshrrev_b32_e32 v18, 5, v6\n"
    "v_readfirstlane_b32 s6, v3\n"
    "v_mov_b32_e32 v3, 0x27fd4\n"
    "v_lshrrev_b32_e32 v0, 1, v0\n"
    "v_mov_b32_e32 v4, s6\n"
    "ds_write_b32 v3, v4\n"
    "v_mov_b32_e32 v3, 0x27ff0\n"
    "ds_write_b32 v3, v2\n"
    "v_mov_b32_e32 v3, 0x7ff00000\n"
    "v_add_u32_e32 v2, 0xc000, v7\n"
    "v_cndmask_b32_e64 v3, 0, v3, s[4:5]\n"
    "v_or_b32_e32 v2, v2, v3\n"
    "s_lshr_b32 s4, s2, 2\n"
    "s_lshl_b32 s2, s2, 3\n"
    "s_waitcnt lgkmcnt(0)\n"
    "s_barrier\n"
    "s_xor_b32 s4, s6, s4\n"
    "s_and_b32 s2, s2, 24\n"
    "v_mov_b32_e32 v3, 0x70000\n"
    "v_mov_b32_e32 v4, 0x7ff70000\n"
    "s_lshl_b32 s4, s4, 13\n"
    "v_add_u32_e32 v2, s2, v6\n"
    "v_cndmask_b32_e64 v3, v3, v4, s[0:1]\n"
    "s_and_b32 s0, s14, 12\n"
    "s_and_b32 s4, s4, 0x2000\n"
    "v_lshl_or_b32 v2, v2, 8, v3\n"
    "s_or_b32 s0, s0, s15\n"
    "v_add_u32_e32 v19, s4, v2\n"
    "v_lshl_or_b32 v2, s0, 3, v8\n"
    "v_or_b32_e32 v2, s4, v2\n"
    "v_or_b32_e32 v17, 0x68000, v2\n"
    "v_and_b32_e32 v2, 0x70, v20\n"
    "v_lshl_or_b32 v2, v18, 7, v2\n"
    "v_and_b32_e32 v0, 12, v0\n"
    "s_mov_b32 s0, 0x22000\n"
    "v_or3_b32 v16, v2, v0, s0\n"
    "s_lshr_b32 s0, s3, 4\n"
    "s_and_b32 s0, s0, 0xffffffc\n"
    "s_add_i32 s0, s0, -16\n"
    "s_lshl_b32 s1, s0, 11\n"
    "s_lshl_b32 s2, s3, 6\n"
    "s_and_b32 s1, s1, 0xffffc000\n"
    "s_and_b32 s2, s2, 0x1000\n"
    "s_or_b32 s1, s1, s2\n"
    "s_or_b32 s1, s1, s4\n"
    "s_lshl_b32 s0, s0, 10\n"
    "s_add_i32 s1, s1, 0x58000\n"
    "s_add_i32 s0, s0, 0x20000\n"
    "v_or_b32_e32 v21, s1, v1\n"
    "v_or_b32_e32 v22, s0, v1\n"
    "s_barrier\n"
    "buffer_load_dwordx4 v[0:3], v21, s[8:11], 0 offen sc1\n"
    "buffer_load_dwordx4 v[4:7], v21, s[8:11], 0 offen offset:1024 sc1\n"
    "buffer_load_dwordx4 v[8:11], v21, s[8:11], 0 offen offset:2048 sc1\n"
    "buffer_load_dwordx4 v[12:15], v21, s[8:11], 0 offen offset:3072 sc1\n"
    "s_waitcnt vmcnt(3)\n"
    "v_max_u32_e32 v23, v0, v1\n"
    "v_max3_u32 v23, v23, v2, v3\n"
    "ds_write_b128 v22, v[0:3]\n"
    "s_waitcnt vmcnt(2)\n"
    "v_max3_u32 v0, v23, v4, v5\n"
    "v_xor_b32_e32 v23, 64, v22\n"
    "v_max3_u32 v0, v0, v6, v7\n"
    "ds_write_b128 v23, v[4:7] offset:1024\n"
    "s_waitcnt vmcnt(1)\n"
    "v_max3_u32 v0, v0, v8, v9\n"
    "v_xor_b32_e32 v24, 0x80, v22\n"
    "v_max3_u32 v0, v0, v10, v11\n"
    "ds_write_b128 v24, v[8:11] offset:2048\n"
    "s_waitcnt vmcnt(0)\n"
    "v_max3_u32 v0, v0, v12, v13\n"
    "v_xor_b32_e32 v25, 0xc0, v22\n"
    "v_max3_u32 v0, v0, v14, v15\n"
    "ds_write_b128 v25, v[12:15] offset:3072\n"
    "v_cmp_ne_u32_e32 vcc, -1, v0\n"
    "s_cmp_eq_u64 vcc, exec\n"
    "s_mov_b32 s4, -1\n"
    "s_cbranch_scc1 .Lfd_R_AH8_15\n"
    "v_add_u32_e32 v26, 0x400, v21\n"
    "v_add_u32_e32 v27, 0x800, v21\n"
    "v_add_u32_e32 v28, 0xc00, v21\n"
    "v_mov_b32_e32 v29, 0x7fff\n"
    ".Lfd_R_AH8_13:\n"
    "buffer_load_dwordx4 v[0:3], v21, s[8:11], 0 offen sc1\n"
    "buffer_load_dwordx4 v[4:7], v26, s[8:11], 0 offen sc1\n"
    "buffer_load_dwordx4 v[8:11], v27, s[8:11], 0 offen sc1\n"
    "buffer_load_dwordx4 v[12:15], v28, s[8:11], 0 offen sc1\n"
    "v_subrev_co_u32_e32 v29, vcc, 1, v29\n"
    "s_waitcnt vmcnt(3)\n"
    "v_max_u32_e32 v30, v0, v1\n"
    "v_max3_u32 v30, v30, v2, v3\n"
    "s_waitcnt vmcnt(2)\n"
    "v_max3_u32 v30, v30, v4, v5\n"
    "v_max3_u32 v30, v30, v6, v7\n"
    "s_waitcnt vmcnt(1)\n"
    "v_max3_u32 v30, v30, v8, v9\n"
    "v_max3_u32 v30, v30, v10, v11\n"
    "s_waitcnt vmcnt(0)\n"
    "v_max3_u32 v30, v30, v12, v13\n"
    "v_max3_u32 v30, v30, v14, v15\n"
    "v_cmp_ne_u32_e64 s[0:1], -1, v30\n"
    "s_cmp_eq_u64 s[0:1], exec\n"
    "s_cselect_b64 s[0:1], -1, 0\n"
    "s_or_b64 s[0:1], s[0:1], vcc\n"
    "s_andn2_b64 vcc, exec, s[0:1]\n"
    "s_cbranch_vccnz .Lfd_R_AH8_13\n"
    "ds_write_b128 v22, v[0:3]\n"
    "ds_write_b128 v23, v[4:7] offset:1024\n"
    "ds_write_b128 v24, v[8:11] offset:2048\n"
    "ds_write_b128 v25, v[12:15] offset:3072\n"
    ".Lfd_R_AH8_15:\n"
    "v_lshlrev_b32_e32 v0, 10, v18\n"
    "v_and_b32_e32 v1, 0x1f0, v20\n"
    "s_mov_b32 s0, 0x24000\n"
    "v_or3_b32 v0, v0, v1, s0\n"
    "s_waitcnt lgkmcnt(0)\n"
    "s_barrier\n"
    "s_barrier\n"
    "s_mov_b32 s5, s4\n"
    "s_mov_b32 s6, s4\n"
    "s_mov_b32 s7, s4\n"
    "v_mov_b64_e32 v[2:3], s[4:5]\n"
    "v_mov_b64_e32 v[4:5], s[6:7]\n"
    "ds_write_b128 v0, v[2:5]\n"
    "ds_write_b128 v0, v[2:5] offset:2048\n"
    "s_setprio 3\n"
    "s_waitcnt lgkmcnt(0)\n"
    "s_barrier\n"
    "s_barrier\n"
    "v_mov_b32_e32 v2, 0\n"
    "v_mov_b32_e32 v4, 0x7fff\n"
    "v_mov_b32_e32 v3, v2\n"
    ".Lfd_R_AH8_16:\n"
    "v_mov_b64_e32 v[0:1], v[2:3]\n"
    "buffer_atomic_add_x2 v[0:1], v19, s[8:11], 0 offen sc0 sc1\n"
    "s_waitcnt vmcnt(0)\n"
    "v_subrev_co_u32_e32 v4, vcc, 1, v4\n"
    "v_cmp_eq_u32_e64 s[0:1], 15, v0\n"
    "s_and_b32 s12, s0, 0xff\n"
    "s_cmp_eq_u64 s[12:13], 0xff\n"
    "s_cselect_b64 s[0:1], -1, 0\n"
    "s_or_b64 s[0:1], s[0:1], vcc\n"
    "s_andn2_b64 vcc, exec, s[0:1]\n"
    "s_cbranch_vccnz .Lfd_R_AH8_16\n"
    "v_readlane_b32 s1, v1, 1\n"
    "v_readlane_b32 s3, v1, 2\n"
    "v_readlane_b32 s0, v1, 0\n"
    "v_mov_b32_e32 v0, s1\n"
    "v_mov_b32_e32 v2, s3\n"
    "s_max_u32 s2, s0, s1\n"
    "v_min3_u32 v0, s0, v0, v2\n"
    "v_readlane_b32 s0, v1, 3\n"
    "v_readlane_b32 s1, v1, 4\n"
    "s_nop 0\n"
    "v_mov_b32_e32 v3, s0\n"
    "v_max3_u32 v2, s2, v2, v3\n"
    "v_mov_b32_e32 v3, s1\n"
    "v_min3_u32 v0, v0, s0, v3\n"
    "v_readlane_b32 s0, v1, 5\n"
    "s_nop 1\n"
    "v_mov_b32_e32 v3, s0\n"
    "v_max3_u32 v2, v2, s1, v3\n"
    "v_readlane_b32 s1, v1, 6\n"
    "s_nop 1\n"
    "v_mov_b32_e32 v3, s1\n"
    "v_min3_u32 v0, v0, s0, v3\n"
    "v_readlane_b32 s0, v1, 7\n"
    "v_mov_b32_e32 v3, 0x27418\n"
    "s_nop 0\n"
    "v_mov_b32_e32 v1, s0\n"
    "v_max3_u32 v1, v2, s1, v1\n"
    "v_subrev_u32_e32 v1, 55, v1\n"
    "v_min3_u32 v2, v0, s0, v1\n"
    "v_not_b32_e32 v1, v2\n"
    "v_mov_b32_e32 v0, 1\n"
    "ds_write_b64 v3, v[0:1]\n"
    "v_readfirstlane_b32 s0, v2\n"
    "s_addk_i32 s0, 0x81\n"
    ".Lfd_R_AH8_18:\n"
    "s_memrealtime s[2:3]\n"
    "s_waitcnt lgkmcnt(0)\n"
    "s_add_i32 s1, s0, s2\n"
    "s_cmpk_lt_u32 s1, 0x12a\n"
    "s_cbranch_scc1 .Lfd_R_AH8_18\n"
    "v_mov_b32_e32 v4, 0x7fff\n"
    "s_movk_i32 s2, 0x4000\n"
    ".Lfd_R_AH8_20:\n"
    "buffer_load_dwordx2 v[0:1], v17, s[8:11], 0 offen sc1\n"
    "buffer_load_dwordx2 v[2:3], v17, s[8:11], s2 offen sc1\n"
    "v_subrev_co_u32_e32 v4, vcc, 1, v4\n"
    "s_waitcnt vmcnt(1)\n"
    "v_max_u32_e32 v5, v0, v1\n"
    "s_waitcnt vmcnt(0)\n"
    "v_max3_u32 v5, v5, v2, v3\n"
    "v_cmp_ne_u32_e64 s[0:1], -1, v5\n"
    "s_cmp_eq_u64 s[0:1], exec\n"
    "s_cselect_b64 s[0:1], -1, 0\n"
    "s_or_b64 s[0:1], s[0:1], vcc\n"
    "s_andn2_b64 vcc, exec, s[0:1]\n"
    "s_cbranch_vccnz .Lfd_R_AH8_20\n"
    "v_maximum3_f32 v4, v0, v2, v2\n"
    "s_nop 1\n"
    "v_mov_b32_dpp v5, v4 row_ror:8 row_mask:0xf bank_mask:0xf bound_ctrl:1\n"
    "v_maximum3_f32 v4, v4, v5, v5\n"
    "s_nop 1\n"
    "v_mov_b32_dpp v5, v4 row_ror:4 row_mask:0xf bank_mask:0xf bound_ctrl:1\n"
    "v_maximum3_f32 v4, v4, v5, v5\n"
    "s_nop 1\n"
    "v_mov_b32_dpp v5, v4 row_ror:2 row_mask:0xf bank_mask:0xf bound_ctrl:1\n"
    "v_maximum3_f32 v4, v4, v5, v5\n"
    "s_nop 1\n"
    "v_mov_b32_dpp v5, v4 row_ror:1 row_mask:0xf bank_mask:0xf bound_ctrl:1\n"
    "v_maximum3_f32 v4, v4, v5, v5\n"
    "v_mov_b32_e32 v5, v4\n"
    "s_nop 1\n"
    "v_permlane16_swap_b32_e32 v4, v5\n"
    "v_maximum3_f32 v4, v4, v5, v5\n"
    "v_mov_b32_e32 v5, v4\n"
    "s_nop 1\n"
    "v_permlane32_swap_b32_e32 v4, v5\n"
    "v_maximum3_f32 v4, v4, v5, v5\n"
    "v_sub_f32_e32 v0, v0, v4\n"
    "v_sub_f32_e32 v2, v2, v4\n"
    "v_exp_f32_e32 v0, v0\n"
    "v_exp_f32_e32 v2, v2\n"
    "s_nop 0\n"
    "v_cvt_pk_bf16_f32 v0, v0, v2\n"
    "v_and_b32_e32 v2, 0xffff, v0\n"
    "v_lshrrev_b32_e32 v4, 16, v0\n"
    "ds_write2st64_b32 v16, v2, v4 offset1:1\n"
    "v_mov_b32_e32 v2, 1\n"
    "v_mov_b32_e32 v4, 0x27404\n"
    "ds_write_b32 v4, v2\n"
    "s_setprio 0\n"
    "v_lshlrev_b32_e32 v2, 16, v0\n"
    "v_and_b32_e32 v0, 0xffff0000, v0\n"
    "v_mul_f32_e32 v0, v3, v0\n"
    "v_fmac_f32_e32 v0, v1, v2\n"
    "s_nop 1\n"
    "v_add_f32_dpp v0, v0, v0 row_ror:8 row_mask:0xf bank_mask:0xf bound_ctrl:1\n"
    "s_nop 1\n"
    "v_add_f32_dpp v0, v0, v0 row_ror:4 row_mask:0xf bank_mask:0xf bound_ctrl:1\n"
    "s_nop 1\n"
    "v_add_f32_dpp v0, v0, v0 row_ror:2 row_mask:0xf bank_mask:0xf bound_ctrl:1\n"
    "s_nop 1\n"
    "v_add_f32_dpp v0, v0, v0 row_ror:1 row_mask:0xf bank_mask:0xf bound_ctrl:1\n"
    "v_mov_b32_e32 v1, v0\n"
    "s_nop 1\n"
    "v_permlane16_swap_b32_e32 v0, v1\n"
    "v_add_f32_e32 v0, v0, v1\n"
    "v_mov_b32_e32 v1, v0\n"
    "s_nop 1\n"
    "v_permlane32_swap_b32_e32 v0, v1\n"
    "v_add_f32_e32 v0, v0, v1\n"
    "v_rcp_f32_e32 v0, v0\n"
    "v_mov_b32_e32 v1, 0x27438\n"
    "ds_write_b32 v1, v0\n"
    "s_endpgm\n"
    ".Lfd_R_AH8_22:\n"
    "s_lshl_b32 s4, s6, 13\n"
    "s_branch .Lfd_R_AH8_2\n"
    ".p2align 7\n"
    ".Lfd_R_SI:\n"
    "v_and_b32_e32 v0, 63, v0\n"
    "v_min_u32_e32 v0, 47, v0\n"
    "v_mov_b32_e32 v1, 0x23000\n"
    "v_lshl_or_b32 v0, v0, 2, v1\n"
    "v_mov_b32_e32 v2, 0x27fc8\n"
    "s_waitcnt vmcnt(0)\n"
    "v_mov_b32 v1, v161\n"
    "v_mov_b32 v3, v162\n"
    "s_nop 0\n"
    "v_lshlrev_b32_e32 v4, 16, v1\n"
    "v_and_b32_e32 v1, 0xffff0000, v1\n"
    "v_mul_f32_e32 v5, 0xbfb8aa3b, v4\n"
    "v_mul_f32_e32 v6, 0xbfb8aa3b, v1\n"
    "v_exp_f32_e32 v5, v5\n"
    "v_exp_f32_e32 v6, v6\n"
    "v_add_f32_e32 v5, 1.0, v5\n"
    "v_add_f32_e32 v6, 1.0, v6\n"
    "v_rcp_f32_e32 v5, v5\n"
    "v_rcp_f32_e32 v6, v6\n"
    "v_mul_f32_e32 v4, v5, v4\n"
    "v_mul_f32_e32 v1, v6, v1\n"
    "v_cvt_pk_bf16_f32 v1, v4, v1\n"
    "ds_write_b32 v0, v1\n"
    "v_add_u32_e32 v0, 1, v3\n"
    "v_mov_b32_e32 v1, 1\n"
    "ds_write_b64 v2, v[0:1]\n"
    "s_branch .Lfd_as2\n"
    ".p2align 7\n"
    ".Lfd_R_LN:\n"
    "v_lshlrev_b32_e32 v0, 2, v0\n"
    "v_and_b32_e32 v0, 0xfc, v0\n"
    "v_or_b32_e32 v0, 0x23100, v0\n"
    "v_mov_b32_e32 v1, 0x27fc0\n"
    "v_mov_b32_e32 v9, 0\n"
    "v_mov_b32_e32 v10, 0\n"
    "s_waitcnt vmcnt(0)\n"
    "v_mov_b32 v2, v162\n"
    "v_mov_b32 v3, v163\n"
    "v_mov_b32 v4, v164\n"
    "v_mov_b32 v5, v165\n"
    "v_mov_b32 v6, v166\n"
    "v_mov_b32 v7, v167\n"
    "v_mov_b32 v8, v161\n"
    "s_and_b32 s0, s2, 6\n"
    "v_dot2c_f32_bf16_e32 v10, 0x3f803f80, v2\n"
    "v_dot2c_f32_bf16_e32 v9, v2, v2\n"
    "v_dot2c_f32_bf16_e32 v10, 0x3f803f80, v3\n"
    "v_dot2c_f32_bf16_e32 v9, v3, v3\n"
    "v_dot2c_f32_bf16_e32 v10, 0x3f803f80, v4\n"
    "v_dot2c_f32_bf16_e32 v9, v4, v4\n"
    "v_dot2c_f32_bf16_e32 v10, 0x3f803f80, v5\n"
    "v_dot2c_f32_bf16_e32 v9, v5, v5\n"
    "v_dot2c_f32_bf16_e32 v10, 0x3f803f80, v6\n"
    "v_dot2c_f32_bf16_e32 v9, v6, v6\n"
    "v_dot2c_f32_bf16_e32 v10, 0x3f803f80, v7\n"
    "v_dot2c_f32_bf16_e32 v9, v7, v7\n"
    "s_cmp_eq_u32 s0, 6\n"
    "s_cselect_b64 s[0:1], -1, 0\n"
    "s_lshr_b32 s2, s2, 3\n"
    "v_permlane32_swap_b32_e32 v10, v9\n"
    "v_add_f32_e32 v2, v10, v9\n"
    "v_mov_b32_e32 v3, v2\n"
    "s_nop 1\n"
    "v_permlane16_swap_b32_e32 v2, v3\n"
    "v_add_f32_e32 v2, v2, v3\n"
    "s_sub_i32 s2, s2, 25\n"
    "s_cmp_lt_u32 s2, -9\n"
    "v_add_f32_dpp v2, v2, v2 row_ror:8 row_mask:0xf bank_mask:0xf bound_ctrl:1\n"
    "v_lshlrev_b32_e32 v4, 16, v8\n"
    "s_cselect_b64 s[2:3], -1, 0\n"
    "v_add_f32_dpp v2, v2, v2 row_ror:4 row_mask:0xf bank_mask:0xf bound_ctrl:1\n"
    "s_or_b64 vcc, s[0:1], s[2:3]\n"
    "s_nop 0\n"
    "v_add_f32_dpp v2, v2, v2 row_ror:2 row_mask:0xf bank_mask:0xf bound_ctrl:1\n"
    "s_nop 1\n"
    "v_add_f32_dpp v2, v2, v2 row_ror:1 row_mask:0xf bank_mask:0xf bound_ctrl:1\n"
    "v_mul_f32_e32 v2, 0x3ae38e39, v2\n"
    "v_mov_b32_e32 v3, v2\n"
    "s_nop 1\n"
    "v_permlane32_swap_b32_e32 v2, v3\n"
    "v_fma_f32 v3, -v2, v2, v3\n"
    "v_maximum3_f32 v3, v3, 0, 0\n"
    "v_add_f32_e32 v3, 0x3727c5ac, v3\n"
    "v_rsq_f32_e32 v3, v3\n"
    "v_sub_f32_e32 v2, v4, v2\n"
    "v_mul_f32_e32 v2, v2, v3\n"
    "v_cndmask_b32_e32 v2, 1.0, v2, vcc\n"
    "ds_write_b32 v0, v2\n"
    "v_mov_b32_e32 v0, 1\n"
    "ds_write_b32 v1, v0\n"
    "s_branch .Lfd_as2\n"
    ".p2align 7\n"
    ".Lfd_R_S1:\n"
    "s_load_dwordx2 s[4:5], s[0:1], 0x30\n"
    "s_and_b32 s0, s2, 7\n"
    "s_lshr_b32 s1, s2, 3\n"
    "s_add_i32 s1, s1, -16\n"
    "s_waitcnt lgkmcnt(0)\n"
    "s_and_b32 s5, s5, 0xffff\n"
    "s_add_i32 s2, s0, 10\n"
    "s_cmp_lt_u32 s0, 6\n"
    "s_cselect_b32 s8, s1, s2\n"
    "s_cselect_b32 s9, s0, s1\n"
    "s_cmp_gt_u32 s8, 8\n"
    "s_cselect_b64 s[0:1], -1, 0\n"
    "v_and_b32_e32 v1, 63, v0\n"
    "s_mov_b32 s7, 0x20000\n"
    "s_mov_b32 s6, 0x600000\n"
    "s_and_b64 s[2:3], s[0:1], exec\n"
    "s_cselect_b32 s2, 0xfffff7, 0\n"
    "s_setprio 3\n"
    "v_bfe_u32 v2, v0, 2, 4\n"
    "v_lshlrev_b32_e32 v3, 6, v0\n"
    "v_lshlrev_b32_e32 v4, 5, v0\n"
    "v_lshlrev_b32_e32 v5, 3, v0\n"
    "v_bfe_u32 v6, v0, 1, 1\n"
    "v_and_b32_e32 v7, 2, v2\n"
    "v_and_b32_e32 v2, 4, v2\n"
    "v_and_b32_e32 v3, 0xc00, v3\n"
    "v_and_b32_e32 v4, 0x180, v4\n"
    "v_and_b32_e32 v5, 8, v5\n"
    "v_or3_b32 v2, v6, v7, v2\n"
    "v_lshlrev_b32_e32 v2, 4, v2\n"
    "v_or3_b32 v3, v3, v5, v4\n"
    "s_mov_b32 s3, 0x20020\n"
    "v_bitop3_b32 v30, v2, s3, v3 bitop3:0x36\n"
    "s_mov_b32 s3, 0x20040\n"
    "v_bitop3_b32 v38, v2, s3, v3 bitop3:0x36\n"
    "s_mov_b32 s3, 0x20060\n"
    "v_bitop3_b32 v46, v2, s3, v3 bitop3:0x36\n"
    "s_cselect_b32 s3, 6, 0\n"
    "s_add_i32 s2, s2, s8\n"
    "s_add_i32 s3, s3, s9\n"
    "s_mulk_i32 s3, 0x900\n"
    "s_lshl_b32 s2, s2, 8\n"
    "v_or_b32_e32 v4, v2, v3\n"
    "v_mov_b32_e32 v2, 0x23000\n"
    "s_add_i32 s2, s3, s2\n"
    "v_and_or_b32 v66, v0, 48, v2\n"
    "v_bfe_i32 v0, v0, 0, 1\n"
    "s_addk_i32 s2, 0x4000\n"
    "v_lshlrev_b32_e32 v1, 2, v1\n"
    "v_and_b32_e32 v0, 0x7ff00000, v0\n"
    "s_cmp_eq_u32 s9, 0\n"
    "v_or3_b32 v62, v0, s2, v1\n"
    "s_cselect_b64 s[2:3], -1, 0\n"
    "s_and_b64 s[0:1], s[0:1], s[2:3]\n"
    "v_or_b32_e32 v22, 0x20000, v4\n"
    "v_or_b32_e32 v64, 0x23100, v1\n"
    "v_mov_b32_e32 v65, 0x27fc0\n"
    "v_cndmask_b32_e64 v63, 0, 1.0, s[0:1]\n"
    "s_waitcnt vmcnt(8)\n"
    "ds_read_b64_tr_b16 v[0:1], v22\n"
    "ds_read_b64_tr_b16 v[2:3], v22 offset:512\n"
    "ds_read_b64_tr_b16 v[4:5], v30\n"
    "ds_read_b64_tr_b16 v[6:7], v30 offset:512\n"
    "ds_read_b64_tr_b16 v[8:9], v38\n"
    "ds_read_b64_tr_b16 v[10:11], v38 offset:512\n"
    "ds_read_b64_tr_b16 v[12:13], v46\n"
    "ds_read_b64_tr_b16 v[14:15], v46 offset:512\n"
    "s_waitcnt vmcnt(4)\n"
    "ds_read_b64_tr_b16 v[16:17], v22 offset:4096\n"
    "ds_read_b64_tr_b16 v[18:19], v22 offset:4608\n"
    "ds_read_b64_tr_b16 v[24:25], v30 offset:4096\n"
    "ds_read_b64_tr_b16 v[26:27], v30 offset:4608\n"
    "ds_read_b64_tr_b16 v[32:33], v38 offset:4096\n"
    "ds_read_b64_tr_b16 v[34:35], v38 offset:4608\n"
    "ds_read_b64_tr_b16 v[40:41], v46 offset:4096\n"
    "ds_read_b64_tr_b16 v[42:43], v46 offset:4608\n"
    "s_waitcnt vmcnt(0)\n"
    "ds_read_b64_tr_b16 v[20:21], v22 offset:8192\n"
    "ds_read_b64_tr_b16 v[22:23], v22 offset:8704\n"
    "ds_read_b64_tr_b16 v[28:29], v30 offset:8192\n"
    "ds_read_b64_tr_b16 v[30:31], v30 offset:8704\n"
    "ds_read_b64_tr_b16 v[36:37], v38 offset:8192\n"
    "ds_read_b64_tr_b16 v[38:39], v38 offset:8704\n"
    "ds_read_b64_tr_b16 v[44:45], v46 offset:8192\n"
    "ds_read_b64_tr_b16 v[46:47], v46 offset:8704\n"
    "v_mov_b32_e32 v67, 0xfffff\n"
    ".Lfd_R_S1_1:\n"
    "ds_read_b64 v[60:61], v65 offset:8\n"
    "s_waitcnt lgkmcnt(3)\n"
    "ds_read_b128 v[56:59], v66\n"
    "s_waitcnt lgkmcnt(3)\n"
    "ds_read_b128 v[52:55], v66 offset:64\n"
    "s_waitcnt lgkmcnt(3)\n"
    "ds_read_b128 v[48:51], v66 offset:128\n"
    "v_subrev_co_u32_e32 v67, vcc, 1, v67\n"
    "s_waitcnt lgkmcnt(3)\n"
    "v_readfirstlane_b32 s0, v61\n"
    "s_cmp_lg_u32 s0, 0\n"
    "s_cselect_b64 s[0:1], -1, 0\n"
    "s_or_b64 s[0:1], s[0:1], vcc\n"
    "s_andn2_b64 vcc, exec, s[0:1]\n"
    "s_cbranch_vccnz .Lfd_R_S1_1\n"
    "s_waitcnt lgkmcnt(2)\n"
    "v_mfma_f32_16x16x32_bf16 v[0:3], v[56:59], v[0:3], 0\n"
    "v_mfma_f32_16x16x32_bf16 v[4:7], v[56:59], v[4:7], 0\n"
    "v_mfma_f32_16x16x32_bf16 v[8:11], v[56:59], v[8:11], 0\n"
    "v_mfma_f32_16x16x32_bf16 v[12:15], v[56:59], v[12:15], 0\n"
    "s_waitcnt lgkmcnt(1)\n"
    "v_mfma_f32_16x16x32_bf16 v[0:3], v[52:55], v[16:19], v[0:3]\n"
    "v_mfma_f32_16x16x32_bf16 v[4:7], v[52:55], v[24:27], v[4:7]\n"
    "v_mfma_f32_16x16x32_bf16 v[16:19], v[52:55], v[32:35], v[8:11]\n"
    "v_mfma_f32_16x16x32_bf16 v[12:15], v[52:55], v[40:43], v[12:15]\n"
    "s_nop 1\n"
    "ds_read_b32 v11, v65\n"
    "ds_read_b32 v10, v64\n"
    "s_waitcnt lgkmcnt(2)\n"
    "v_mfma_f32_16x16x32_bf16 v[0:3], v[48:51], v[20:23], v[0:3]\n"
    "v_mfma_f32_16x16x32_bf16 v[2:5], v[48:51], v[28:31], v[4:7]\n"
    "v_mfma_f32_16x16x32_bf16 v[4:7], v[48:51], v[36:39], v[16:19]\n"
    "v_mfma_f32_16x16x32_bf16 v[6:9], v[48:51], v[44:47], v[12:15]\n"
    "s_nop 4\n"
    "v_mov_b32_e32 v1, 0x100000\n"
    "s_branch .Lfd_R_S1_4\n"
    ".Lfd_R_S1_3:\n"
    "s_andn2_b64 vcc, exec, s[0:1]\n"
    "s_cbranch_vccz .Lfd_R_S1_6\n"
    ".Lfd_R_S1_4:\n"
    "s_waitcnt lgkmcnt(1)\n"
    "v_readfirstlane_b32 s0, v11\n"
    "s_cmp_lg_u32 s0, 0\n"
    "s_mov_b64 s[0:1], -1\n"
    "s_cbranch_scc1 .Lfd_R_S1_3\n"
    "ds_read_b32 v11, v65\n"
    "s_waitcnt lgkmcnt(1)\n"
    "ds_read_b32 v10, v64\n"
    "v_add_u32_e32 v1, -1, v1\n"
    "v_cmp_eq_u32_e64 s[0:1], 0, v1\n"
    "s_branch .Lfd_R_S1_3\n"
    ".Lfd_R_S1_6:\n"
    "s_mov_b32 vcc_lo, 0xffff0000\n"
    "s_mov_b32 vcc_hi, vcc_lo\n"
    "s_nop 0\n"
    "v_cndmask_b32_e32 v0, v0, v2, vcc\n"
    "v_cndmask_b32_e32 v1, v4, v6, vcc\n"
    "s_mov_b32 vcc_lo, 0\n"
    "s_mov_b32 vcc_hi, -1\n"
    "v_cndmask_b32_e32 v0, v0, v1, vcc\n"
    "v_add_f32_e32 v0, v63, v0\n"
    "s_waitcnt lgkmcnt(0)\n"
    "v_mul_f32_e32 v0, v10, v0\n"
    "s_nop 1\n"
    "v_mov_b32_dpp v1, v0 quad_perm:[1,0,3,2] row_mask:0xf bank_mask:0xf bound_ctrl:1\n"
    "v_cvt_pk_bf16_f32 v0, v0, v1\n"
    "v_mov_b32_e32 v1, v60\n"
    "buffer_store_dwordx2 v[0:1], v62, s[4:7], 0 offen sc1\n"
    "s_setprio 0\n"
    "s_branch .Lfd_as2\n"
    ".p2align 7\n"
    ".Lfd_R_QT:\n"
    "s_waitcnt vmcnt(0)\n"
    "v_mov_b32 v2, v162\n"
    "v_mov_b32 v1, v163\n"
    "s_waitcnt lgkmcnt(0)\n"
    "s_mov_b32 s2, s94\n"
    "s_memrealtime s[4:5]\n"
    "s_waitcnt lgkmcnt(0)\n"
    "s_sub_i32 s3, s4, s2\n"
    "s_cmpk_gt_u32 s3, 0xa9\n"
    "s_cbranch_scc1 .Lfd_R_QT_2\n"
    ".Lfd_R_QT_1:\n"
    "s_sleep 2\n"
    "s_memrealtime s[4:5]\n"
    "s_waitcnt lgkmcnt(0)\n"
    "s_sub_i32 s3, s4, s2\n"
    "s_cmpk_lt_u32 s3, 0xaa\n"
    "s_cbranch_scc1 .Lfd_R_QT_1\n"
    ".Lfd_R_QT_2:\n"
    "s_load_dwordx2 s[4:5], s[0:1], 0x30\n"
    "v_mov_b32_e32 v3, 0\n"
    "v_lshlrev_b32_e32 v0, 7, v0\n"
    "v_lshl_add_u64 v[2:3], v[2:3], 0, 1\n"
    "v_and_b32_e32 v0, 0x1f80, v0\n"
    "s_mov_b32 s7, 0x20000\n"
    "s_mov_b32 s6, 0x600000\n"
    "s_waitcnt lgkmcnt(0)\n"
    "s_and_b32 s5, s5, 0xffff\n"
    "v_or_b32_e32 v4, 0x2000, v0\n"
    "v_add_u32_e32 v3, v1, v3\n"
    "s_mov_b32 s2, 0x8000\n"
    "s_brev_b32 s3, -2\n"
    "s_branch .Lfd_R_QT_4\n"
    ".Lfd_R_QT_3:\n"
    "s_andn2_b64 vcc, exec, s[0:1]\n"
    "s_cbranch_vccz .Lfd_R_QT_6\n"
    ".Lfd_R_QT_4:\n"
    "buffer_load_dwordx2 v[0:1], v4, s[4:7], 0 offen sc1\n"
    "s_waitcnt vmcnt(0)\n"
    "v_cmp_eq_u32_e32 vcc, v0, v2\n"
    "v_bitop3_b32 v0, v1, s3, v3 bitop3:0x48\n"
    "v_cmp_eq_u32_e64 s[0:1], 0, v0\n"
    "s_and_b64 s[0:1], vcc, s[0:1]\n"
    "s_nop 0\n"
    "v_cndmask_b32_e64 v0, 0, 1, s[0:1]\n"
    "v_cmp_ne_u32_e32 vcc, 0, v0\n"
    "s_cmp_eq_u64 vcc, exec\n"
    "s_mov_b64 s[0:1], -1\n"
    "s_cbranch_scc1 .Lfd_R_QT_3\n"
    "s_add_i32 s2, s2, -1\n"
    "s_cmp_eq_u32 s2, 0\n"
    "s_cselect_b64 s[0:1], -1, 0\n"
    "s_sleep 1\n"
    "s_branch .Lfd_R_QT_3\n"
    ".Lfd_R_QT_6:\n"
    "v_cmp_gt_i32_e32 vcc, 0, v1\n"
    "s_and_b32 s3, vcc_hi, 0xc00000\n"
    "s_and_b32 s2, vcc_lo, 0xc00000c0\n"
    "s_mov_b32 s1, 0\n"
    "s_bcnt1_i32_b64 s0, s[2:3]\n"
    "v_cmp_gt_u64_e64 s[2:3], s[0:1], 3\n"
    "v_mov_b32_e32 v2, 0x27fd0\n"
    "s_nop 0\n"
    "v_cndmask_b32_e64 v0, 0, 1, s[2:3]\n"
    "s_and_b32 s3, vcc_hi, 0xc00000c0\n"
    "s_and_b32 s2, vcc_lo, 0xc000\n"
    "s_bcnt1_i32_b64 s0, s[2:3]\n"
    "v_cmp_gt_u64_e64 s[2:3], s[0:1], 3\n"
    "s_and_b64 s[2:3], s[2:3], exec\n"
    "s_cselect_b32 s0, 2, 0\n"
    "s_and_b32 s3, vcc_hi, 0xc000\n"
    "s_and_b32 s2, vcc_lo, 0xc00000\n"
    "v_or_b32_e32 v0, s0, v0\n"
    "s_bcnt1_i32_b64 s0, s[2:3]\n"
    "v_cmp_gt_u64_e64 s[0:1], s[0:1], 2\n"
    "s_and_b64 s[0:1], s[0:1], exec\n"
    "s_cselect_b32 s0, 4, 0\n"
    "v_or_b32_e32 v0, s0, v0\n"
    "s_nop 0\n"
    "v_readfirstlane_b32 s0, v0\n"
    "s_lshl_b32 s1, s0, 13\n"
    "s_and_b32 s1, s1, 0x2000\n"
    "v_mov_b32_e32 v0, s0\n"
    "v_mov_b32_e32 v1, s1\n"
    "ds_write_b64 v2, v[0:1]\n"
    "s_waitcnt lgkmcnt(0)\n"
    "s_barrier\n"
    "s_setprio 0\n"
    "s_branch .Lfd_pf2\n"
    ".p2align 7\n"
    ".Lfd_R_HG:\n"
    "s_load_dwordx2 s[4:5], s[0:1], 0x30\n"
    "v_readfirstlane_b32 s0, v0\n"
    "s_lshr_b32 s0, s0, 6\n"
    "s_lshr_b32 s1, s2, 3\n"
    "s_mul_i32 s1, s1, 7\n"
    "s_lshl_b32 s2, s0, 8\n"
    "s_mul_i32 s0, s0, 6\n"
    "s_add_i32 s0, s0, s1\n"
    "s_addk_i32 s0, 0xff97\n"
    "v_and_b32_e32 v1, 31, v0\n"
    "s_lshr_b32 s0, s0, 3\n"
    "v_bfe_u32 v0, v0, 5, 1\n"
    "s_min_u32 s0, s0, 5\n"
    "v_min_u32_e32 v2, 23, v1\n"
    "v_mul_u32_u24_e32 v1, 0x900, v0\n"
    "v_lshlrev_b32_e32 v3, 4, v2\n"
    "s_mulk_i32 s0, 0x180\n"
    "v_add3_u32 v1, v3, v1, s0\n"
    "s_add_i32 s2, s2, 0x24700\n"
    "v_lshlrev_b32_e32 v2, 3, v2\n"
    "v_lshlrev_b32_e32 v0, 2, v0\n"
    "v_add_u32_e32 v1, 0x4000, v1\n"
    "v_or3_b32 v0, s2, v2, v0\n"
    "s_waitcnt vmcnt(0)\n"
    "v_mov_b32 v2, v162\n"
    "v_mov_b32 v3, v163\n"
    "s_mov_b32 s7, 0x20000\n"
    "s_mov_b32 s6, 0x600000\n"
    "s_waitcnt lgkmcnt(0)\n"
    "s_and_b32 s5, s5, 0xffff\n"
    "v_add_u32_e32 v2, 1, v2\n"
    "v_mov_b32_e32 v3, 0x3f80\n"
    "v_mov_b32_e32 v4, 1.0\n"
    "s_setprio 3\n"
    "s_waitcnt lgkmcnt(0)\n"
    "s_mov_b32 s0, s94\n"
    "s_memrealtime s[2:3]\n"
    "s_waitcnt lgkmcnt(0)\n"
    "s_sub_i32 s1, s2, s0\n"
    "s_cmpk_gt_u32 s1, 0xa9\n"
    "s_cbranch_scc1 .Lfd_R_HG_2\n"
    ".Lfd_R_HG_1:\n"
    "s_sleep 2\n"
    "s_memrealtime s[2:3]\n"
    "s_waitcnt lgkmcnt(0)\n"
    "s_sub_i32 s1, s2, s0\n"
    "s_cmpk_lt_u32 s1, 0xaa\n"
    "s_cbranch_scc1 .Lfd_R_HG_1\n"
    ".Lfd_R_HG_2:\n"
    "v_mov_b32_e32 v5, 0x7fff\n"
    "s_movk_i32 s2, 0x1200\n"
    "s_movk_i32 s3, 0x2400\n"
    "s_movk_i32 s8, 0x3600\n"
    "s_movk_i32 s9, 0x4800\n"
    "s_movk_i32 s10, 0x5a00\n"
    ".Lfd_R_HG_3:\n"
    "buffer_load_dwordx4 v[10:13], v1, s[4:7], 0 offen sc1\n"
    "buffer_load_dwordx4 v[14:17], v1, s[4:7], s2 offen sc1\n"
    "buffer_load_dwordx4 v[18:21], v1, s[4:7], s3 offen sc1\n"
    "buffer_load_dwordx4 v[22:25], v1, s[4:7], s8 offen sc1\n"
    "buffer_load_dwordx4 v[26:29], v1, s[4:7], s9 offen sc1\n"
    "buffer_load_dwordx4 v[30:33], v1, s[4:7], s10 offen sc1\n"
    "v_mov_b32_e32 v7, 0\n"
    "v_mov_b32_e32 v6, 0\n"
    "v_mov_b32_e32 v9, 0\n"
    "v_mov_b32_e32 v8, 0\n"
    "s_waitcnt vmcnt(5)\n"
    "v_dot2c_f32_bf16_e32 v7, v10, v3\n"
    "v_dot2c_f32_bf16_e32 v6, v10, v4\n"
    "v_dot2c_f32_bf16_e32 v9, v12, v3\n"
    "v_dot2c_f32_bf16_e32 v8, v12, v4\n"
    "s_waitcnt vmcnt(4)\n"
    "v_dot2c_f32_bf16_e32 v7, v14, v3\n"
    "v_dot2c_f32_bf16_e32 v6, v14, v4\n"
    "v_dot2c_f32_bf16_e32 v9, v16, v3\n"
    "v_dot2c_f32_bf16_e32 v8, v16, v4\n"
    "s_waitcnt vmcnt(3)\n"
    "v_dot2c_f32_bf16_e32 v7, v18, v3\n"
    "v_dot2c_f32_bf16_e32 v6, v18, v4\n"
    "v_dot2c_f32_bf16_e32 v9, v20, v3\n"
    "v_dot2c_f32_bf16_e32 v8, v20, v4\n"
    "s_waitcnt vmcnt(2)\n"
    "v_dot2c_f32_bf16_e32 v7, v22, v3\n"
    "v_dot2c_f32_bf16_e32 v6, v22, v4\n"
    "v_dot2c_f32_bf16_e32 v9, v24, v3\n"
    "v_dot2c_f32_bf16_e32 v8, v24, v4\n"
    "s_waitcnt vmcnt(1)\n"
    "v_dot2c_f32_bf16_e32 v7, v26, v3\n"
    "v_dot2c_f32_bf16_e32 v6, v26, v4\n"
    "v_dot2c_f32_bf16_e32 v9, v28, v3\n"
    "v_dot2c_f32_bf16_e32 v8, v28, v4\n"
    "s_waitcnt vmcnt(0)\n"
    "v_dot2c_f32_bf16_e32 v7, v30, v3\n"
    "v_dot2c_f32_bf16_e32 v6, v30, v4\n"
    "v_dot2c_f32_bf16_e32 v9, v32, v3\n"
    "v_dot2c_f32_bf16_e32 v8, v32, v4\n"
    "v_cmp_eq_u32_e32 vcc, v13, v2\n"
    "v_cmp_eq_u32_e64 s[0:1], v11, v2\n"
    "s_and_b64 s[0:1], vcc, s[0:1]\n"
    "v_cmp_eq_u32_e32 vcc, v17, v2\n"
    "s_and_b64 s[0:1], s[0:1], vcc\n"
    "v_cmp_eq_u32_e32 vcc, v15, v2\n"
    "s_and_b64 s[0:1], s[0:1], vcc\n"
    "v_cmp_eq_u32_e32 vcc, v21, v2\n"
    "s_and_b64 s[0:1], s[0:1], vcc\n"
    "v_cmp_eq_u32_e32 vcc, v19, v2\n"
    "s_and_b64 s[0:1], s[0:1], vcc\n"
    "v_cmp_eq_u32_e32 vcc, v25, v2\n"
    "s_and_b64 s[0:1], s[0:1], vcc\n"
    "v_cmp_eq_u32_e32 vcc, v23, v2\n"
    "s_and_b64 s[0:1], s[0:1], vcc\n"
    "v_cmp_eq_u32_e32 vcc, v29, v2\n"
    "s_and_b64 s[0:1], s[0:1], vcc\n"
    "v_cmp_eq_u32_e32 vcc, v27, v2\n"
    "s_and_b64 s[0:1], s[0:1], vcc\n"
    "v_cmp_eq_u32_e32 vcc, v33, v2\n"
    "s_and_b64 s[0:1], s[0:1], vcc\n"
    "v_cmp_eq_u32_e32 vcc, v31, v2\n"
    "s_and_b64 s[0:1], s[0:1], vcc\n"
    "v_cndmask_b32_e64 v10, 0, 1, s[0:1]\n"
    "v_cmp_ne_u32_e32 vcc, 0, v10\n"
    "s_cmp_eq_u64 vcc, exec\n"
    "s_cselect_b64 s[0:1], -1, 0\n"
    "v_subrev_co_u32_e32 v5, vcc, 1, v5\n"
    "s_or_b64 s[0:1], s[0:1], vcc\n"
    "s_andn2_b64 vcc, exec, s[0:1]\n"
    "s_cbranch_vccnz .Lfd_R_HG_3\n"
    "v_permlane32_swap_b32_e32 v7, v9\n"
    "v_permlane32_swap_b32_e32 v6, v8\n"
    "v_add_f32_e32 v1, v7, v9\n"
    "v_add_f32_e32 v2, v6, v8\n"
    "v_cvt_pk_bf16_f32 v1, v1, v2\n"
    "ds_write_b32 v0, v1\n"
    "s_waitcnt lgkmcnt(0)\n"
    "s_barrier\n"
    "s_setprio 0\n"
    "s_branch .Lfd_pf2\n"
    ".p2align 7\n"
    ".Lfd_R_S2:\n"
    "s_load_dwordx2 s[4:5], s[0:1], 0x30\n"
    "s_lshr_b32 s1, s2, 3\n"
    "v_readfirstlane_b32 s0, v0\n"
    "s_mul_i32 s1, s1, 7\n"
    "s_lshr_b32 s0, s0, 6\n"
    "s_sub_i32 s1, s1, 63\n"
    "v_bfe_u32 v2, v0, 2, 4\n"
    "v_lshlrev_b32_e32 v3, 6, v0\n"
    "s_add_i32 s3, s0, s1\n"
    "s_mulk_i32 s0, 0x3000\n"
    "v_and_b32_e32 v3, 0xc00, v3\n"
    "v_lshlrev_b32_e32 v4, 5, v0\n"
    "v_lshlrev_b32_e32 v5, 3, v0\n"
    "v_bfe_u32 v6, v0, 1, 1\n"
    "v_and_b32_e32 v7, 2, v2\n"
    "v_and_b32_e32 v2, 4, v2\n"
    "v_and_b32_e32 v4, 0x180, v4\n"
    "v_and_b32_e32 v5, 8, v5\n"
    "v_or3_b32 v2, v6, v7, v2\n"
    "v_or_b32_e32 v3, s0, v3\n"
    "v_lshlrev_b32_e32 v2, 4, v2\n"
    "v_or3_b32 v3, v3, v5, v4\n"
    "s_movk_i32 s0, 0x60\n"
    "s_lshr_b32 s8, s3, 3\n"
    "v_bitop3_b32 v46, v3, s0, v2 bitop3:0x36\n"
    "s_lshr_b32 s0, s1, 3\n"
    "s_sub_i32 s0, s8, s0\n"
    "s_lshl_b32 s0, s0, 8\n"
    "s_add_i32 s0, s0, 0x24e00\n"
    "v_and_or_b32 v56, v0, 48, s0\n"
    "s_lshl_b32 s0, s2, 2\n"
    "s_and_b32 s0, s0, 12\n"
    "s_bfe_u32 s1, s3, 0x20001\n"
    "s_or_b32 s0, s1, s0\n"
    "s_lshl_b32 s1, s2, 11\n"
    "v_and_b32_e32 v1, 63, v0\n"
    "s_and_b32 s1, s1, 0x2000\n"
    "s_lshl_b32 s2, s3, 8\n"
    "v_lshl_or_b32 v0, v1, 2, s1\n"
    "s_and_b32 s2, s2, 0x100\n"
    "v_lshl_add_u32 v0, s8, 9, v0\n"
    "v_or_b32_e32 v0, s2, v0\n"
    "v_lshl_add_u32 v0, s0, 14, v0\n"
    "v_or_b32_e32 v34, v3, v2\n"
    "v_bitop3_b32 v38, v3, 32, v2 bitop3:0x36\n"
    "v_bitop3_b32 v42, v3, 64, v2 bitop3:0x36\n"
    "v_add_u32_e32 v60, 0x11200, v0\n"
    "v_mov_b32_e32 v61, 0x27fd4\n"
    "s_waitcnt lgkmcnt(0)\n"
    "s_and_b32 s5, s5, 0xffff\n"
    "s_mov_b32 s7, 0x20000\n"
    "s_mov_b32 s6, 0x600000\n"
    "s_waitcnt vmcnt(0)\n"
    "ds_read_b64_tr_b16 v[0:1], v34\n"
    "ds_read_b64_tr_b16 v[2:3], v34 offset:512\n"
    "ds_read_b64_tr_b16 v[4:5], v34 offset:4096\n"
    "ds_read_b64_tr_b16 v[6:7], v34 offset:4608\n"
    "ds_read_b64_tr_b16 v[8:9], v38\n"
    "ds_read_b64_tr_b16 v[10:11], v38 offset:512\n"
    "ds_read_b64_tr_b16 v[12:13], v38 offset:4096\n"
    "ds_read_b64_tr_b16 v[14:15], v38 offset:4608\n"
    "ds_read_b64_tr_b16 v[16:17], v42\n"
    "ds_read_b64_tr_b16 v[18:19], v42 offset:512\n"
    "ds_read_b64_tr_b16 v[20:21], v42 offset:4096\n"
    "ds_read_b64_tr_b16 v[22:23], v42 offset:4608\n"
    "ds_read_b64_tr_b16 v[24:25], v46\n"
    "ds_read_b64_tr_b16 v[26:27], v46 offset:512\n"
    "ds_read_b64_tr_b16 v[28:29], v46 offset:4096\n"
    "ds_read_b64_tr_b16 v[30:31], v46 offset:4608\n"
    "ds_read_b64_tr_b16 v[32:33], v34 offset:8192\n"
    "ds_read_b64_tr_b16 v[34:35], v34 offset:8704\n"
    "ds_read_b64_tr_b16 v[36:37], v38 offset:8192\n"
    "ds_read_b64_tr_b16 v[38:39], v38 offset:8704\n"
    "ds_read_b64_tr_b16 v[40:41], v42 offset:8192\n"
    "ds_read_b64_tr_b16 v[42:43], v42 offset:8704\n"
    "ds_read_b64_tr_b16 v[44:45], v46 offset:8192\n"
    "ds_read_b64_tr_b16 v[46:47], v46 offset:8704\n"
    "s_setprio 3\n"
    "s_waitcnt lgkmcnt(0)\n"
    "s_barrier\n"
    "ds_read_b128 v[48:51], v56\n"
    "ds_read_b128 v[52:55], v56 offset:64\n"
    "ds_read_b128 v[56:59], v56 offset:128\n"
    "ds_read_b32 v61, v61\n"
    "s_waitcnt lgkmcnt(3)\n"
    "v_mfma_f32_16x16x32_bf16 v[0:3], v[48:51], v[0:3], 0\n"
    "s_mov_b32 vcc_lo, 0xffff0000\n"
    "s_mov_b32 s0, 0\n"
    "s_mov_b32 vcc_hi, vcc_lo\n"
    "v_mfma_f32_16x16x32_bf16 v[8:11], v[48:51], v[8:11], 0\n"
    "s_mov_b32 s1, -1\n"
    "v_mfma_f32_16x16x32_bf16 v[16:19], v[48:51], v[16:19], 0\n"
    "v_mfma_f32_16x16x32_bf16 v[24:27], v[48:51], v[24:27], 0\n"
    "s_waitcnt lgkmcnt(2)\n"
    "v_mfma_f32_16x16x32_bf16 v[0:3], v[52:55], v[4:7], v[0:3]\n"
    "v_mfma_f32_16x16x32_bf16 v[4:7], v[52:55], v[12:15], v[8:11]\n"
    "v_mfma_f32_16x16x32_bf16 v[8:11], v[52:55], v[20:23], v[16:19]\n"
    "v_mfma_f32_16x16x32_bf16 v[12:15], v[52:55], v[28:31], v[24:27]\n"
    "s_waitcnt lgkmcnt(1)\n"
    "v_mfma_f32_16x16x32_bf16 v[0:3], v[56:59], v[32:35], v[0:3]\n"
    "v_mfma_f32_16x16x32_bf16 v[2:5], v[56:59], v[36:39], v[4:7]\n"
    "s_waitcnt lgkmcnt(0)\n"
    "s_nop 5\n"
    "v_xor_b32_e32 v1, v61, v60\n"
    "v_mfma_f32_16x16x32_bf16 v[4:7], v[56:59], v[40:43], v[8:11]\n"
    "v_cndmask_b32_e32 v0, v0, v2, vcc\n"
    "v_mfma_f32_16x16x32_bf16 v[6:9], v[56:59], v[44:47], v[12:15]\n"
    "s_nop 7\n"
    "v_cndmask_b32_e32 v2, v4, v6, vcc\n"
    "v_cndmask_b32_e64 v0, v0, v2, s[0:1]\n"
    "buffer_store_dword v0, v1, s[4:7], 0 offen sc1\n"
    "s_setprio 0\n"
    "s_branch .Lfd_pf2\n"
    ".p2align 7\n"
    ".Lfd_R_PF:\n"
    "s_lshr_b32 s8, s2, 3\n"
    "s_add_i32 s8, s8, -9\n"
    "s_cmp_gt_u32 s8, 3\n"
    "v_readfirstlane_b32 s3, v0\n"
    "s_cbranch_scc1 .Lfd_R_PF_4\n"
    "s_waitcnt lgkmcnt(0)\n"
    "s_mov_b32 s9, s94\n"
    "s_memrealtime s[10:11]\n"
    "s_load_dwordx4 s[4:7], s[0:1], 0x48\n"
    "s_waitcnt lgkmcnt(0)\n"
    "s_sub_i32 s0, s10, s9\n"
    "s_cmpk_gt_u32 s0, 0x1c1\n"
    "s_cbranch_scc1 .Lfd_R_PF_3\n"
    ".Lfd_R_PF_2:\n"
    "s_sleep 2\n"
    "s_memrealtime s[0:1]\n"
    "s_waitcnt lgkmcnt(0)\n"
    "s_sub_i32 s0, s0, s9\n"
    "s_cmpk_lt_u32 s0, 0x1c2\n"
    "s_cbranch_scc1 .Lfd_R_PF_2\n"
    ".Lfd_R_PF_3:\n"
    "s_and_b32 s1, s2, 7\n"
    "s_lshr_b32 s2, s3, 6\n"
    "s_add_i32 s9, s2, -6\n"
    "s_cmpk_gt_u32 s3, 0x17f\n"
    "s_cselect_b32 s0, s6, s4\n"
    "s_cselect_b32 s3, s7, s5\n"
    "s_mul_i32 s1, s1, 9\n"
    "s_cselect_b32 s4, s9, s2\n"
    "s_lshr_b32 s2, s0, 7\n"
    "s_add_i32 s2, s1, s2\n"
    "s_mulk_i32 s4, 0x60\n"
    "s_lshl_b32 s7, s8, 3\n"
    "s_sub_i32 s2, 3, s2\n"
    "s_or_b32 s4, s4, s7\n"
    "v_lshrrev_b32_e32 v1, 3, v0\n"
    "s_and_b32 s6, s2, 7\n"
    "v_and_or_b32 v1, v1, 7, s4\n"
    "s_movk_i32 s4, 0x2400\n"
    "s_add_i32 s5, s6, s1\n"
    "v_mul_lo_u32 v1, v1, s4\n"
    "v_lshl_add_u32 v1, s5, 7, v1\n"
    "v_lshlrev_b32_e32 v0, 4, v0\n"
    "s_movk_i32 s4, 0x70\n"
    "s_and_b32 s1, s3, 0xffff\n"
    "s_mov_b32 s3, 0x20000\n"
    "s_mov_b32 s2, 0x510000\n"
    "v_and_or_b32 v0, v0, s4, v1\n"
    "s_mov_b32 s4, 0x48000\n"
    "s_mov_b32 s5, 0x90000\n"
    "buffer_load_dwordx4 v[164:167], v0, s[0:3], s4 offen\n"
    "buffer_load_dwordx4 v[164:167], v0, s[0:3], s5 offen\n"
    "s_cmp_lg_u32 s6, 0\n"
    "s_cbranch_scc0 .Lfd_R_PF_5\n"
    ".Lfd_R_PF_4:\n"
    "s_branch .Lfd_p2\n"
    ".Lfd_R_PF_5:\n"
    "buffer_load_dwordx4 v[164:167], v0, s[0:3], s4 offen offset:1024\n"
    "buffer_load_dwordx4 v[164:167], v0, s[0:3], s5 offen offset:1024\n"
    "s_branch .Lfd_p2\n"
    ".p2align 6\n"
    ".Lfd_p2:\n"
    "v_readfirstlane_b32 s3, v160\n"
    "s_mov_b64 s[0:1], s[96:97]\n"
    "s_mov_b32 s2, s98\n"
    "v_mov_b32 v0, v160\n"
    "s_lshr_b32 s3, s3, 6\n"
    "s_cmp_ge_u32 s2, 126\n"
    "s_cbranch_scc1 .Lfd_p2x\n"
    "s_cmp_ge_u32 s2, 112\n"
    "s_cbranch_scc1 .Lfd_R_M\n"
    "s_and_b32 s4, s2, 0x79\n"
    "s_cmp_eq_u32 s4, 0x69\n"
    "s_cbranch_scc1 .Lfd_R_M\n"
    "s_cmp_lt_u32 s3, 8\n"
    "s_cbranch_scc1 .Lfd_R_O\n"
    "s_cmp_eq_u32 s3, 8\n"
    "s_cbranch_scc1 .Lfd_R_O8\n"
    "s_cmp_eq_u32 s3, 9\n"
    "s_cbranch_scc1 .Lfd_R_R\n"
    "s_barrier\n"
    ".Lfd_p2x:\n"
    "s_endpgm\n"
    ".p2align 7\n"
    ".Lfd_R_FG:\n"
    "v_and_b32_e32 v28, 63, v0\n"
    "v_readfirstlane_b32 s12, v0\n"
    "v_bfe_u32 v26, v0, 4, 2\n"
    "v_bfe_u32 v1, v0, 2, 4\n"
    "v_lshlrev_b32_e32 v3, 5, v0\n"
    "v_lshlrev_b32_e32 v29, 3, v28\n"
    "s_lshr_b32 s13, s12, 6\n"
    "v_lshlrev_b32_e32 v2, 10, v26\n"
    "v_and_b32_e32 v3, 0x180, v3\n"
    "v_and_b32_e32 v4, 8, v29\n"
    "v_bfe_u32 v5, v0, 1, 1\n"
    "v_and_b32_e32 v6, 2, v1\n"
    "v_and_b32_e32 v1, 4, v1\n"
    "s_lshr_b32 s16, s2, 3\n"
    "v_or3_b32 v1, v5, v6, v1\n"
    "v_or3_b32 v2, v2, v4, v3\n"
    "s_mul_i32 s6, s13, 9\n"
    "s_and_b32 s14, s2, 7\n"
    "v_and_b32_e32 v27, 15, v0\n"
    "s_mul_i32 s15, s13, 0x3000\n"
    "v_lshl_or_b32 v2, v1, 4, v2\n"
    "v_mov_b32_e32 v3, 0x60\n"
    "s_add_i32 s6, s6, s16\n"
    "s_lshl_b32 s17, s14, 2\n"
    "v_bitop3_b32 v60, s15, v3, v2 bitop3:0x36\n"
    "s_lshr_b32 s7, s6, 4\n"
    "s_lshl_b32 s18, s6, 5\n"
    "v_xor_b32_e32 v3, v26, v27\n"
    "v_or_b32_e32 v1, s15, v2\n"
    "v_bitop3_b32 v58, s15, 32, v2 bitop3:0x36\n"
    "v_bitop3_b32 v59, s15, 64, v2 bitop3:0x36\n"
    "s_cmp_lt_u32 s6, 64\n"
    "v_lshlrev_b32_e32 v2, 4, v3\n"
    "s_cbranch_scc1 .Lfd_R_FG_2\n"
    "v_lshlrev_b32_e32 v3, 4, v3\n"
    "s_mov_b64 s[4:5], 0\n"
    "s_branch .Lfd_R_FG_3\n"
    ".Lfd_R_FG_2:\n"
    "s_mov_b64 s[4:5], -1\n"
    ".Lfd_R_FG_3:\n"
    "s_load_dwordx2 s[10:11], s[0:1], 0x58\n"
    "s_load_dwordx2 s[8:9], s[0:1], 0x20\n"
    "s_load_dwordx2 s[2:3], s[0:1], 0x48\n"
    "s_add_i32 s17, s7, s17\n"
    "s_andn2_b64 vcc, exec, s[4:5]\n"
    "s_and_b32 s18, s18, 0x1e0\n"
    "s_cbranch_vccnz .Lfd_R_FG_5\n"
    "v_or_b32_e32 v3, s18, v26\n"
    "s_lshl_b32 s19, s17, 8\n"
    "v_lshl_or_b32 v3, v3, 13, s19\n"
    "s_movk_i32 s19, 0x80\n"
    "v_bitop3_b32 v7, v3, s19, v2 bitop3:0x36\n"
    "s_movk_i32 s19, 0xc0\n"
    "v_or_b32_e32 v4, v3, v2\n"
    "v_bitop3_b32 v5, v3, 64, v2 bitop3:0x36\n"
    "v_bitop3_b32 v3, v3, s19, v2 bitop3:0x36\n"
    "s_load_dwordx2 s[4:5], s[0:1], 0x18\n"
    "v_add_u32_e32 v6, 0x7c00, v5\n"
    "v_add_u32_e32 v8, 0xf800, v7\n"
    "v_add_u32_e32 v9, 0x17400, v3\n"
    "s_waitcnt lgkmcnt(0)\n"
    "s_add_u32 m0, s15, 0\n"
    "s_nop 0\n"
    "global_load_lds_dwordx4 v4, s[4:5] nt\n"
    "global_load_lds_dwordx4 v6, s[4:5] offset:1024 nt\n"
    "global_load_lds_dwordx4 v8, s[4:5] offset:2048 nt\n"
    "global_load_lds_dwordx4 v9, s[4:5] offset:3072 nt\n"
    "v_or_b32_e32 v4, 0x20000, v4\n"
    "v_add_u32_e32 v5, 0x27c00, v5\n"
    "v_add_u32_e32 v6, 0x2f800, v7\n"
    "v_add_u32_e32 v3, 0x37400, v3\n"
    "s_add_u32 m0, s15, 0x1000\n"
    "s_nop 0\n"
    "global_load_lds_dwordx4 v4, s[4:5] nt\n"
    "global_load_lds_dwordx4 v5, s[4:5] offset:1024 nt\n"
    "global_load_lds_dwordx4 v6, s[4:5] offset:2048 nt\n"
    "global_load_lds_dwordx4 v3, s[4:5] offset:3072 nt\n"
    "v_mov_b32_e32 v3, v2\n"
    ".Lfd_R_FG_5:\n"
    "s_load_dwordx2 s[4:5], s[0:1], 0x30\n"
    "s_lshl_b32 s0, s7, 8\n"
    "s_and_b32 s0, s0, 0x300\n"
    "v_lshlrev_b32_e32 v2, 8, v27\n"
    "v_and_b32_e32 v31, 48, v0\n"
    "s_waitcnt lgkmcnt(0)\n"
    "s_and_b32 s5, s5, 0xffff\n"
    "s_cmp_gt_u32 s6, 63\n"
    "v_add3_u32 v33, v3, v2, s15\n"
    "v_or_b32_e32 v2, s0, v31\n"
    "s_cselect_b64 s[0:1], -1, 0\n"
    "s_lshl_b32 s6, s17, 11\n"
    "s_and_b32 s6, s6, 0xffffc000\n"
    "v_lshrrev_b32_e32 v30, 5, v28\n"
    "v_or_b32_e32 v54, 0x24000, v2\n"
    "v_and_b32_e32 v2, 1, v0\n"
    "s_add_i32 s6, s6, 0x58000\n"
    "v_and_b32_e32 v84, 31, v0\n"
    "v_cmp_eq_u32_e32 vcc, 1, v2\n"
    "v_lshl_or_b32 v2, v30, 13, s6\n"
    "s_lshl_b32 s6, s17, 10\n"
    "s_and_b32 s6, s6, 0x1c00\n"
    "v_or_b32_e32 v3, s18, v84\n"
    "v_mov_b32_e32 v32, 0x7ff00000\n"
    "s_or_b64 vcc, vcc, s[0:1]\n"
    "v_or_b32_e32 v2, s6, v2\n"
    "v_lshlrev_b32_e32 v3, 1, v3\n"
    "v_cndmask_b32_e32 v4, 0, v32, vcc\n"
    "v_or3_b32 v61, v3, v2, v4\n"
    "s_waitcnt vmcnt(0)\n"
    "ds_read_b128 v[2:5], v33\n"
    "ds_read_b128 v[6:9], v33 offset:4096\n"
    "v_xor_b32_e32 v14, 64, v33\n"
    "v_xor_b32_e32 v22, 0x80, v33\n"
    "v_xor_b32_e32 v33, 0xc0, v33\n"
    "ds_read_b128 v[10:13], v14\n"
    "ds_read_b128 v[14:17], v14 offset:4096\n"
    "ds_read_b128 v[18:21], v22\n"
    "ds_read_b128 v[22:25], v22 offset:4096\n"
    "ds_read_b128 v[34:37], v33\n"
    "ds_read_b128 v[38:41], v33 offset:4096\n"
    "s_waitcnt lgkmcnt(0)\n"
    "s_waitcnt lgkmcnt(0)\n"
    "s_mov_b32 s7, 0x20000\n"
    "s_mov_b32 s6, 0x600000\n"
    "s_setprio 3\n"
    "s_waitcnt lgkmcnt(0)\n"
    "s_barrier\n"
    "ds_read_b128 v[42:45], v54\n"
    "ds_read_b128 v[46:49], v54 offset:64\n"
    "ds_read_b128 v[50:53], v54 offset:128\n"
    "ds_read_b128 v[54:57], v54 offset:192\n"
    "s_waitcnt lgkmcnt(3)\n"
    "v_mfma_f32_16x16x32_bf16 v[2:5], v[42:45], v[2:5], 0\n"
    "s_mov_b32 vcc_lo, 0xffff0000\n"
    "s_mov_b32 vcc_hi, vcc_lo\n"
    "v_mfma_f32_16x16x32_bf16 v[6:9], v[42:45], v[6:9], 0\n"
    "s_waitcnt lgkmcnt(2)\n"
    "v_mfma_f32_16x16x32_bf16 v[2:5], v[46:49], v[10:13], v[2:5]\n"
    "v_mfma_f32_16x16x32_bf16 v[6:9], v[46:49], v[14:17], v[6:9]\n"
    "s_waitcnt lgkmcnt(1)\n"
    "v_mfma_f32_16x16x32_bf16 v[2:5], v[50:53], v[18:21], v[2:5]\n"
    "v_mfma_f32_16x16x32_bf16 v[6:9], v[50:53], v[22:25], v[6:9]\n"
    "s_waitcnt lgkmcnt(0)\n"
    "v_mfma_f32_16x16x32_bf16 v[2:5], v[54:57], v[34:37], v[2:5]\n"
    "v_mfma_f32_16x16x32_bf16 v[4:7], v[54:57], v[38:41], v[6:9]\n"
    "s_nop 7\n"
    "v_cndmask_b32_e32 v2, v2, v4, vcc\n"
    "v_mul_f32_e32 v2, 0x3e0293ee, v2\n"
    "s_nop 1\n"
    "v_mov_b32_dpp v3, v2 quad_perm:[1,0,3,2] row_mask:0xf bank_mask:0xf bound_ctrl:1\n"
    "v_cvt_pk_bf16_f32 v2, v2, v3\n"
    "buffer_store_dword v2, v61, s[4:7], 0 offen sc1\n"
    "s_setprio 0\n"
    "s_barrier\n"
    "s_waitcnt lgkmcnt(0)\n"
    "s_mov_b32 s0, s94\n"
    "s_memrealtime s[18:19]\n"
    "s_waitcnt lgkmcnt(0)\n"
    "s_sub_i32 s1, s18, s0\n"
    "s_cmpk_gt_u32 s1, 0x1fd\n"
    "s_cbranch_scc1 .Lfd_R_FG_7\n"
    ".Lfd_R_FG_6:\n"
    "s_sleep 2\n"
    "s_memrealtime s[18:19]\n"
    "s_waitcnt lgkmcnt(0)\n"
    "s_sub_i32 s1, s18, s0\n"
    "s_cmpk_lt_u32 s1, 0x1fe\n"
    "s_cbranch_scc1 .Lfd_R_FG_6\n"
    ".Lfd_R_FG_7:\n"
    "s_mul_i32 s0, s14, 9\n"
    "v_lshlrev_b32_e32 v4, 4, v0\n"
    "s_add_i32 s0, s0, s16\n"
    "v_lshrrev_b32_e32 v2, 3, v28\n"
    "v_and_b32_e32 v4, 0x70, v4\n"
    "v_lshlrev_b32_e32 v33, 1, v28\n"
    "s_mul_i32 s1, s0, 0x12000\n"
    "v_mul_u32_u24_e32 v3, 0x480, v2\n"
    "v_bitop3_b32 v6, v33, v4, 32 bitop3:0x6c\n"
    "v_or3_b32 v3, s1, v3, v6\n"
    "v_mul_u32_u24_e32 v2, 0x2400, v2\n"
    "v_and_b32_e32 v5, 32, v33\n"
    "v_lshl_add_u32 v3, s13, 7, v3\n"
    "v_lshl_add_u32 v2, s0, 7, v2\n"
    "v_xor_b32_e32 v6, 64, v3\n"
    "s_mul_i32 s1, s13, 0xd8000\n"
    "v_bitop3_b32 v2, v2, v5, v4 bitop3:0xf6\n"
    "v_add_u32_e32 v7, 0x2000, v6\n"
    "v_add_u32_e32 v8, 0x4000, v3\n"
    "v_add_u32_e32 v9, 0x6000, v6\n"
    "s_add_u32 m0, s15, 0x1000\n"
    "s_nop 0\n"
    "global_load_lds_dwordx4 v3, s[10:11] nt\n"
    "global_load_lds_dwordx4 v7, s[10:11] offset:1024 nt\n"
    "global_load_lds_dwordx4 v8, s[10:11] offset:2048 nt\n"
    "global_load_lds_dwordx4 v9, s[10:11] offset:3072 nt\n"
    "v_add_u32_e32 v34, s1, v2\n"
    "v_add_u32_e32 v7, 0x9000, v3\n"
    "v_add_u32_e32 v8, 0xb000, v6\n"
    "v_add_u32_e32 v3, 0xd000, v3\n"
    "v_add_u32_e32 v6, 0xf000, v6\n"
    "s_add_u32 m0, s15, 0x2000\n"
    "s_nop 0\n"
    "global_load_lds_dwordx4 v7, s[10:11] nt\n"
    "global_load_lds_dwordx4 v8, s[10:11] offset:1024 nt\n"
    "global_load_lds_dwordx4 v3, s[10:11] offset:2048 nt\n"
    "global_load_lds_dwordx4 v6, s[10:11] offset:3072 nt\n"
    "v_xor_b32_e32 v35, 64, v34\n"
    "s_mul_i32 s1, s13, 3\n"
    "v_add_u32_e32 v2, 0x11c00, v35\n"
    "s_barrier\n"
    "v_subrev_u32_e32 v18, s15, v1\n"
    "s_mul_i32 s11, s13, 0x60\n"
    "v_mov_b32_e32 v19, 0x60\n"
    "s_lshr_b32 s10, s1, 2\n"
    "v_add_u32_e32 v3, 0x23800, v34\n"
    "v_add_u32_e32 v4, 0x35400, v35\n"
    "s_add_u32 m0, s15, 0\n"
    "s_nop 0\n"
    "global_load_lds_dwordx4 v34, s[2:3] nt\n"
    "global_load_lds_dwordx4 v2, s[2:3] offset:1024 nt\n"
    "global_load_lds_dwordx4 v3, s[2:3] offset:2048 nt\n"
    "global_load_lds_dwordx4 v4, s[2:3] offset:3072 nt\n"
    "v_bitop3_b32 v2, s11, v18, v19 bitop3:0x6c\n"
    "s_mulk_i32 s10, 0x3000\n"
    "v_add_u32_e32 v8, s10, v2\n"
    "s_add_i32 s10, s1, 1\n"
    "s_lshl_b32 s16, s10, 5\n"
    "s_lshr_b32 s10, s10, 2\n"
    "v_bitop3_b32 v10, s16, v18, v19 bitop3:0x6c\n"
    "s_mulk_i32 s10, 0x3000\n"
    "v_add_u32_e32 v16, s10, v10\n"
    "s_add_i32 s10, s1, 2\n"
    "s_lshl_b32 s16, s10, 5\n"
    "s_lshr_b32 s10, s10, 2\n"
    "s_waitcnt vmcnt(4)\n"
    "v_bitop3_b32 v18, s16, v18, v19 bitop3:0x6c\n"
    "s_mulk_i32 s10, 0x3000\n"
    "s_barrier\n"
    "v_add_u32_e32 v20, s10, v18\n"
    "ds_read_b64_tr_b16 v[2:3], v8 offset:4096\n"
    "ds_read_b64_tr_b16 v[4:5], v8 offset:4608\n"
    "ds_read_b64_tr_b16 v[6:7], v8 offset:8192\n"
    "ds_read_b64_tr_b16 v[8:9], v8 offset:8704\n"
    "ds_read_b64_tr_b16 v[10:11], v16 offset:4096\n"
    "ds_read_b64_tr_b16 v[12:13], v16 offset:4608\n"
    "ds_read_b64_tr_b16 v[14:15], v16 offset:8192\n"
    "ds_read_b64_tr_b16 v[16:17], v16 offset:8704\n"
    "ds_read_b64_tr_b16 v[22:23], v20 offset:4096\n"
    "ds_read_b64_tr_b16 v[24:25], v20 offset:4608\n"
    "ds_read_b64_tr_b16 v[18:19], v20 offset:8192\n"
    "ds_read_b64_tr_b16 v[20:21], v20 offset:8704\n"
    "s_waitcnt lgkmcnt(0)\n"
    "s_waitcnt lgkmcnt(0)\n"
    "s_barrier\n"
    "v_add_u32_e32 v36, 0x48000, v34\n"
    "v_add_u32_e32 v37, 0x59c00, v35\n"
    "v_add_u32_e32 v38, 0x6b800, v34\n"
    "v_add_u32_e32 v39, 0x7d400, v35\n"
    "s_add_u32 m0, s15, 0x1000\n"
    "s_nop 0\n"
    "global_load_lds_dwordx4 v36, s[2:3] nt\n"
    "global_load_lds_dwordx4 v37, s[2:3] offset:1024 nt\n"
    "global_load_lds_dwordx4 v38, s[2:3] offset:2048 nt\n"
    "global_load_lds_dwordx4 v39, s[2:3] offset:3072 nt\n"
    "s_barrier\n"
    "v_and_b32_e32 v0, 3, v0\n"
    "v_add_u32_e32 v36, 0x90000, v34\n"
    "v_add_u32_e32 v37, 0xa1c00, v35\n"
    "v_add_u32_e32 v34, 0xb3800, v34\n"
    "v_add_u32_e32 v35, 0xc5400, v35\n"
    "s_add_u32 m0, s15, 0x2000\n"
    "s_nop 0\n"
    "global_load_lds_dwordx4 v36, s[2:3] nt\n"
    "global_load_lds_dwordx4 v37, s[2:3] offset:1024 nt\n"
    "global_load_lds_dwordx4 v34, s[2:3] offset:2048 nt\n"
    "global_load_lds_dwordx4 v35, s[2:3] offset:3072 nt\n"
    "s_mul_i32 s2, s13, 0x280\n"
    "v_min_u32_e32 v0, 2, v0\n"
    "s_add_i32 s2, s2, 0x24e00\n"
    "v_mul_u32_u24_e32 v0, 0xd0, v0\n"
    "s_and_b32 s3, s12, 0x1fffffc0\n"
    "v_add3_u32 v88, v0, v31, s2\n"
    "v_or_b32_e32 v0, s3, v28\n"
    "v_mov_b32_e32 v28, 0x26c00\n"
    "v_mov_b32_e32 v87, 0x27800\n"
    "v_lshl_add_u32 v86, v0, 3, v28\n"
    "v_or_b32_e32 v85, 0x26c00, v29\n"
    "v_or_b32_e32 v83, 0x25d00, v33\n"
    "v_or_b32_e32 v0, 0x25d00, v31\n"
    "v_mov_b32_e32 v28, 0x27fd4\n"
    "ds_read_b32 v28, v28\n"
    "v_add_u32_e32 v29, s1, v26\n"
    "v_lshlrev_b32_e32 v31, 14, v29\n"
    "v_add_u32_e32 v33, 0xfffba000, v31\n"
    "v_cmp_lt_u32_e32 vcc, 17, v29\n"
    "v_lshlrev_b32_e32 v27, 2, v27\n"
    "v_lshl_or_b32 v27, s0, 6, v27\n"
    "v_cndmask_b32_e32 v29, v31, v33, vcc\n"
    "s_mov_b32 s0, 0x10000\n"
    "v_cmp_eq_u32_e32 vcc, 3, v26\n"
    "v_add3_u32 v27, v27, v29, s0\n"
    "v_min_u32_e32 v93, 23, v84\n"
    "v_cndmask_b32_e32 v26, 0, v32, vcc\n"
    "s_lshl_b32 s0, s14, 11\n"
    "s_waitcnt lgkmcnt(0)\n"
    "v_bitop3_b32 v82, v28, v26, v27 bitop3:0xde\n"
    "v_lshl_add_u32 v26, v93, 2, s11\n"
    "s_and_b32 s0, s0, 0x2000\n"
    "v_mul_u32_u24_e32 v27, 0x900, v30\n"
    "v_lshlrev_b32_e32 v29, 2, v26\n"
    "v_or_b32_e32 v27, s0, v27\n"
    "s_mov_b32 s1, 0x80000\n"
    "v_lshlrev_b32_e32 v95, 2, v30\n"
    "v_add3_u32 v27, v29, v27, s1\n"
    "v_lshl_or_b32 v26, v26, 1, v95\n"
    "v_xor_b32_e32 v89, v28, v27\n"
    "v_add_u32_e32 v27, s0, v26\n"
    "v_add_u32_e32 v27, 0x88000, v27\n"
    "s_movk_i32 s10, 0x480\n"
    "v_xor_b32_e32 v61, v28, v27\n"
    "s_and_b32 s9, s9, 0xffff\n"
    "s_mov_b32 s11, s7\n"
    "s_waitcnt vmcnt(4)\n"
    "buffer_load_dword v96, v26, s[8:11], 0 offen\n"
    "ds_read_b64_tr_b16 v[46:47], v1\n"
    "ds_read_b64_tr_b16 v[48:49], v1 offset:512\n"
    "ds_read_b64_tr_b16 v[54:55], v1 offset:4096\n"
    "ds_read_b64_tr_b16 v[56:57], v1 offset:4608\n"
    "ds_read_b64_tr_b16 v[50:51], v58\n"
    "ds_read_b64_tr_b16 v[52:53], v58 offset:512\n"
    "ds_read_b64_tr_b16 v[42:43], v58 offset:4096\n"
    "ds_read_b64_tr_b16 v[44:45], v58 offset:4608\n"
    "ds_read_b64_tr_b16 v[38:39], v59\n"
    "ds_read_b64_tr_b16 v[40:41], v59 offset:512\n"
    "ds_read_b64_tr_b16 v[34:35], v59 offset:4096\n"
    "ds_read_b64_tr_b16 v[36:37], v59 offset:4608\n"
    "ds_read_b64_tr_b16 v[30:31], v60\n"
    "ds_read_b64_tr_b16 v[32:33], v60 offset:512\n"
    "ds_read_b64_tr_b16 v[26:27], v60 offset:4096\n"
    "ds_read_b64_tr_b16 v[28:29], v60 offset:4608\n"
    "s_waitcnt lgkmcnt(0)\n"
    "s_mov_b32 s0, s94\n"
    "s_memrealtime s[8:9]\n"
    "s_waitcnt lgkmcnt(0)\n"
    "s_sub_i32 s1, s8, s0\n"
    "s_cmpk_gt_u32 s1, 0x3b5\n"
    "s_cbranch_scc1 .Lfd_R_FG_9\n"
    ".Lfd_R_FG_8:\n"
    "s_sleep 2\n"
    "s_memrealtime s[8:9]\n"
    "s_waitcnt lgkmcnt(0)\n"
    "s_sub_i32 s1, s8, s0\n"
    "s_cmpk_lt_u32 s1, 0x3b6\n"
    "s_cbranch_scc1 .Lfd_R_FG_8\n"
    ".Lfd_R_FG_9:\n"
    "v_add_u32_e32 v62, 0x480, v61\n"
    "v_add_u32_e32 v63, 0x900, v61\n"
    "s_mov_b32 s3, 0x8000\n"
    "s_branch .Lfd_R_FG_11\n"
    ".Lfd_R_FG_10:\n"
    "s_andn2_b64 vcc, exec, s[0:1]\n"
    "s_cbranch_vccz .Lfd_R_FG_13\n"
    ".Lfd_R_FG_11:\n"
    "buffer_load_dword v92, v61, s[4:7], 0 offen sc1\n"
    "buffer_load_dword v97, v62, s[4:7], 0 offen sc1\n"
    "buffer_load_dword v90, v63, s[4:7], 0 offen sc1\n"
    "s_mov_b64 s[0:1], -1\n"
    "s_waitcnt vmcnt(0)\n"
    "v_max3_u32 v64, v92, v97, v90\n"
    "v_cmp_ne_u32_e32 vcc, -1, v64\n"
    "s_cmp_eq_u64 vcc, exec\n"
    "s_cbranch_scc1 .Lfd_R_FG_10\n"
    "s_add_i32 s3, s3, -1\n"
    "s_cmp_eq_u32 s3, 0\n"
    "s_cselect_b64 s[0:1], -1, 0\n"
    "s_sleep 4\n"
    "s_branch .Lfd_R_FG_10\n"
    ".Lfd_R_FG_13:\n"
    "buffer_load_dwordx4 v[74:77], v89, s[4:7], 0 offen sc1\n"
    "s_movk_i32 s3, 0x4000\n"
    "buffer_load_dwordx4 v[78:81], v89, s[4:7], s3 offen sc1\n"
    "ds_read_b64_tr_b16 v[70:71], v1 offset:8192\n"
    "ds_read_b64_tr_b16 v[72:73], v1 offset:8704\n"
    "ds_read_b64_tr_b16 v[66:67], v58 offset:8192\n"
    "ds_read_b64_tr_b16 v[68:69], v58 offset:8704\n"
    "ds_read_b64_tr_b16 v[62:63], v59 offset:8192\n"
    "ds_read_b64_tr_b16 v[64:65], v59 offset:8704\n"
    "ds_read_b64_tr_b16 v[58:59], v60 offset:8192\n"
    "ds_read_b64_tr_b16 v[60:61], v60 offset:8704\n"
    "v_lshlrev_b32_e32 v1, 16, v90\n"
    "v_and_b32_e32 v90, 0xffff0000, v90\n"
    "v_add_f32_e32 v1, 1.0, v1\n"
    "v_add_f32_e32 v90, 1.0, v90\n"
    "s_lshl_b32 s0, s13, 3\n"
    "v_cvt_pk_bf16_f32 v98, v1, v90\n"
    "v_lshl_add_u32 v1, v93, 3, s2\n"
    "s_add_i32 s0, s0, 0x27800\n"
    "v_or_b32_e32 v93, v1, v95\n"
    "v_or_b32_e32 v1, s0, v95\n"
    "v_mov_b32_e32 v95, 0x3ae38e39\n"
    "v_cmp_gt_u32_e32 vcc, 24, v84\n"
    "v_lshlrev_b32_e32 v90, 16, v98\n"
    "v_and_b32_e32 v91, 0xffff0000, v98\n"
    "v_lshlrev_b32_e32 v94, 16, v92\n"
    "v_and_b32_e32 v92, 0xffff0000, v92\n"
    "v_cndmask_b32_e32 v84, 0, v95, vcc\n"
    "ds_write2_b32 v93, v98, v97 offset0:52 offset1:104\n"
    "v_lshlrev_b32_e32 v95, 16, v96\n"
    "v_and_b32_e32 v96, 0xffff0000, v96\n"
    "s_waitcnt vmcnt(1)\n"
    "v_max_u32_e32 v97, v74, v75\n"
    "v_max3_u32 v97, v97, v76, v77\n"
    "s_waitcnt vmcnt(0)\n"
    "v_max3_u32 v97, v97, v78, v79\n"
    "v_max3_u32 v97, v97, v80, v81\n"
    "v_cmp_ne_u32_e32 vcc, -1, v97\n"
    "s_cmp_eq_u64 vcc, exec\n"
    "s_cbranch_scc1 .Lfd_R_FG_16\n"
    "v_mov_b32_e32 v97, 0x7fff\n"
    ".Lfd_R_FG_15:\n"
    "buffer_load_dwordx4 v[74:77], v89, s[4:7], 0 offen sc1\n"
    "buffer_load_dwordx4 v[78:81], v89, s[4:7], s3 offen sc1\n"
    "v_subrev_co_u32_e32 v97, vcc, 1, v97\n"
    "s_waitcnt vmcnt(1)\n"
    "v_max_u32_e32 v98, v74, v75\n"
    "v_max3_u32 v98, v98, v76, v77\n"
    "s_waitcnt vmcnt(0)\n"
    "v_max3_u32 v98, v98, v78, v79\n"
    "v_max3_u32 v98, v98, v80, v81\n"
    "v_cmp_ne_u32_e64 s[0:1], -1, v98\n"
    "s_cmp_eq_u64 s[0:1], exec\n"
    "s_cselect_b64 s[0:1], -1, 0\n"
    "s_or_b64 s[0:1], s[0:1], vcc\n"
    "s_andn2_b64 vcc, exec, s[0:1]\n"
    "s_cbranch_vccnz .Lfd_R_FG_15\n"
    ".Lfd_R_FG_16:\n"
    "v_pk_add_f32 v[76:77], v[80:81], v[76:77]\n"
    "v_pk_add_f32 v[74:75], v[78:79], v[74:75]\n"
    "s_nop 1\n"
    "v_permlane32_swap_b32_e32 v74, v76\n"
    "v_add_f32_e32 v74, v74, v76\n"
    "v_permlane32_swap_b32_e32 v75, v77\n"
    "v_fmac_f32_e32 v95, v94, v74\n"
    "v_add_f32_e32 v74, v75, v77\n"
    "v_fmac_f32_e32 v96, v92, v74\n"
    "v_mul_f32_e32 v74, v90, v95\n"
    "v_mul_f32_e32 v75, v91, v96\n"
    "v_cvt_pk_bf16_f32 v74, v74, v75\n"
    "ds_write_b32 v93, v74\n"
    "v_mul_f32_e32 v75, v96, v96\n"
    "v_add_f32_e32 v74, v95, v96\n"
    "v_fmac_f32_e32 v75, v95, v95\n"
    "v_mul_f32_e32 v74, v84, v74\n"
    "v_mul_f32_e32 v75, v84, v75\n"
    "s_nop 1\n"
    "v_permlane32_swap_b32_e32 v74, v75\n"
    "v_add_f32_e32 v74, v74, v75\n"
    "v_mov_b32_e32 v75, v74\n"
    "s_nop 1\n"
    "v_permlane16_swap_b32_e32 v74, v75\n"
    "v_add_f32_e32 v74, v74, v75\n"
    "s_nop 1\n"
    "v_add_f32_dpp v74, v74, v74 row_ror:8 row_mask:0xf bank_mask:0xf bound_ctrl:1\n"
    "s_nop 1\n"
    "v_add_f32_dpp v74, v74, v74 row_ror:4 row_mask:0xf bank_mask:0xf bound_ctrl:1\n"
    "s_nop 1\n"
    "v_add_f32_dpp v74, v74, v74 row_ror:2 row_mask:0xf bank_mask:0xf bound_ctrl:1\n"
    "s_nop 1\n"
    "v_add_f32_dpp v74, v74, v74 row_ror:1 row_mask:0xf bank_mask:0xf bound_ctrl:1\n"
    "ds_write_b32 v1, v74\n"
    "s_waitcnt lgkmcnt(0)\n"
    "s_barrier\n"
    "ds_read_b128 v[74:77], v88\n"
    "ds_read_b128 v[78:81], v88 offset:64\n"
    "ds_read_b128 v[88:91], v88 offset:128\n"
    "ds_read_b128 v[92:95], v87\n"
    "ds_read_b128 v[96:99], v87 offset:16\n"
    "ds_read_b128 v[100:103], v87 offset:32\n"
    "s_mov_b32 s0, 0\n"
    "s_mov_b32 s1, -1\n"
    "s_waitcnt lgkmcnt(5)\n"
    "v_mfma_f32_16x16x32_bf16 v[46:49], v[74:77], v[46:49], 0\n"
    "s_waitcnt lgkmcnt(4)\n"
    "v_mfma_f32_16x16x32_bf16 v[46:49], v[78:81], v[54:57], v[46:49]\n"
    "s_waitcnt lgkmcnt(3)\n"
    "v_mfma_f32_16x16x32_bf16 v[46:49], v[88:91], v[70:73], v[46:49]\n"
    "s_waitcnt lgkmcnt(2)\n"
    "v_add_f32_e32 v1, v92, v94\n"
    "s_waitcnt lgkmcnt(1)\n"
    "s_nop 4\n"
    "v_add_f32_e32 v49, v96, v98\n"
    "v_mfma_f32_16x16x32_bf16 v[50:53], v[74:77], v[50:53], 0\n"
    "s_waitcnt lgkmcnt(0)\n"
    "v_add_f32_e32 v54, v100, v102\n"
    "v_add_f32_e32 v1, v1, v49\n"
    "v_mfma_f32_16x16x32_bf16 v[42:45], v[78:81], v[42:45], v[50:53]\n"
    "v_add_f32_e32 v49, v93, v95\n"
    "s_nop 2\n"
    "v_add_f32_e32 v50, v97, v99\n"
    "v_mfma_f32_16x16x32_bf16 v[42:45], v[88:91], v[66:69], v[42:45]\n"
    "s_nop 7\n"
    "v_add_f32_e32 v45, v101, v103\n"
    "v_add_f32_e32 v1, v1, v54\n"
    "v_mfma_f32_16x16x32_bf16 v[38:41], v[74:77], v[38:41], 0\n"
    "v_add_f32_e32 v49, v49, v50\n"
    "v_fma_f32 v45, -v1, v1, v45\n"
    "v_mfma_f32_16x16x32_bf16 v[34:37], v[78:81], v[34:37], v[38:41]\n"
    "s_nop 4\n"
    "v_add_f32_e32 v38, v49, v45\n"
    "v_maximum3_f32 v38, v38, 0, 0\n"
    "v_mfma_f32_16x16x32_bf16 v[34:37], v[88:91], v[62:65], v[34:37]\n"
    "s_nop 7\n"
    "v_add_f32_e32 v37, 0x3727c5ac, v38\n"
    "v_rsq_f32_e32 v37, v37\n"
    "v_mfma_f32_16x16x32_bf16 v[30:33], v[74:77], v[30:33], 0\n"
    "v_fma_f32 v38, -v1, v47, v46\n"
    "v_fmac_f32_e32 v48, v37, v38\n"
    "v_mfma_f32_16x16x32_bf16 v[26:29], v[78:81], v[26:29], v[30:33]\n"
    "s_nop 4\n"
    "v_fma_f32 v30, -v1, v43, v42\n"
    "v_fmac_f32_e32 v44, v37, v30\n"
    "v_mfma_f32_16x16x32_bf16 v[26:29], v[88:91], v[58:61], v[26:29]\n"
    "s_mov_b32 vcc_lo, 0xffff0000\n"
    "s_mov_b32 vcc_hi, vcc_lo\n"
    "s_nop 5\n"
    "v_cndmask_b32_e32 v29, v48, v44, vcc\n"
    "v_fma_f32 v30, -v1, v35, v34\n"
    "v_fmac_f32_e32 v36, v37, v30\n"
    "v_fma_f32 v1, -v1, v27, v26\n"
    "v_fmac_f32_e32 v28, v37, v1\n"
    "v_cndmask_b32_e32 v1, v36, v28, vcc\n"
    "v_cndmask_b32_e64 v1, v29, v1, s[0:1]\n"
    "ds_write_b32 v86, v1\n"
    "s_waitcnt lgkmcnt(0)\n"
    "s_barrier\n"
    "s_cmp_gt_u32 s12, 63\n"
    "s_cbranch_scc1 .Lfd_R_FG_18\n"
    "ds_read2st64_b64 v[26:29], v85 offset1:1\n"
    "ds_read2st64_b64 v[30:33], v85 offset0:2 offset1:3\n"
    "ds_read2st64_b64 v[34:37], v85 offset0:4 offset1:5\n"
    "s_waitcnt lgkmcnt(2)\n"
    "v_pk_add_f32 v[26:27], v[26:27], v[28:29]\n"
    "s_waitcnt lgkmcnt(1)\n"
    "v_pk_add_f32 v[28:29], v[30:31], v[32:33]\n"
    "s_nop 0\n"
    "v_pk_add_f32 v[26:27], v[26:27], v[28:29]\n"
    "s_waitcnt lgkmcnt(0)\n"
    "v_pk_add_f32 v[28:29], v[34:35], v[36:37]\n"
    "s_nop 0\n"
    "v_pk_add_f32 v[26:27], v[26:27], v[28:29]\n"
    "v_mov_b32_e32 v28, 0xc0135761\n"
    "v_mul_f32_e32 v1, v26, v26\n"
    "v_fmac_f32_e32 v28, 0xbdd2d3e7, v1\n"
    "v_mul_f32_e32 v1, v26, v28\n"
    "v_exp_f32_e32 v1, v1\n"
    "v_mul_f32_e32 v26, v26, v27\n"
    "v_add_f32_e32 v1, 1.0, v1\n"
    "v_rcp_f32_e32 v1, v1\n"
    "s_nop 0\n"
    "v_mul_f32_e32 v1, v26, v1\n"
    "v_cvt_pk_bf16_f32 v1, v1, s0\n"
    "ds_write_b16 v83, v1\n"
    ".Lfd_R_FG_18:\n"
    "s_waitcnt lgkmcnt(0)\n"
    "s_barrier\n"
    "ds_read_b128 v[26:29], v0\n"
    "ds_read_b128 v[30:33], v0 offset:64\n"
    "s_waitcnt lgkmcnt(1)\n"
    "v_mfma_f32_16x16x32_bf16 v[0:3], v[26:29], v[2:5], 0\n"
    "s_xor_b64 s[2:3], s[0:1], -1\n"
    "s_waitcnt lgkmcnt(0)\n"
    "v_mfma_f32_16x16x32_bf16 v[0:3], v[30:33], v[6:9], v[0:3]\n"
    "v_mfma_f32_16x16x32_bf16 v[2:5], v[26:29], v[10:13], 0\n"
    "v_mfma_f32_16x16x32_bf16 v[2:5], v[30:33], v[14:17], v[2:5]\n"
    "v_mfma_f32_16x16x32_bf16 v[4:7], v[26:29], v[22:25], 0\n"
    "v_mfma_f32_16x16x32_bf16 v[4:7], v[30:33], v[18:21], v[4:7]\n"
    "s_and_saveexec_b64 s[0:1], s[2:3]\n"
    "s_mov_b32 vcc_hi, vcc_lo\n"
    "s_nop 5\n"
    "v_cndmask_b32_e32 v4, v0, v2, vcc\n"
    "s_or_b64 exec, exec, s[0:1]\n"
    "buffer_store_dword v4, v82, s[4:7], 0 offen sc1\n"
    "s_endpgm\n"
    ".p2align 7\n"
    ".Lfd_R_FU:\n"
    "v_and_b32_e32 v2, 63, v0\n"
    "v_readfirstlane_b32 s3, v0\n"
    "s_lshr_b32 s12, s3, 6\n"
    "v_bfe_u32 v29, v0, 4, 2\n"
    "v_bfe_u32 v1, v0, 2, 4\n"
    "v_lshlrev_b32_e32 v4, 5, v0\n"
    "v_lshlrev_b32_e32 v31, 3, v2\n"
    "s_lshr_b32 s15, s2, 3\n"
    "v_lshlrev_b32_e32 v3, 10, v29\n"
    "v_and_b32_e32 v4, 0x180, v4\n"
    "v_and_b32_e32 v5, 8, v31\n"
    "v_bfe_u32 v6, v0, 1, 1\n"
    "v_and_b32_e32 v7, 2, v1\n"
    "v_and_b32_e32 v1, 4, v1\n"
    "s_mul_i32 s6, s12, 9\n"
    "s_and_b32 s14, s2, 7\n"
    "v_and_b32_e32 v30, 15, v0\n"
    "v_or3_b32 v1, v6, v7, v1\n"
    "v_or3_b32 v3, v3, v5, v4\n"
    "s_add_i32 s6, s6, s15\n"
    "s_lshl_b32 s4, s14, 2\n"
    "s_mul_i32 s13, s12, 0x3000\n"
    "v_lshl_or_b32 v1, v1, 4, v3\n"
    "v_mov_b32_e32 v3, 0x60\n"
    "s_lshr_b32 s16, s6, 4\n"
    "s_lshl_b32 s5, s6, 5\n"
    "v_xor_b32_e32 v4, v29, v30\n"
    "v_or_b32_e32 v28, s13, v1\n"
    "v_bitop3_b32 v27, s13, 32, v1 bitop3:0x36\n"
    "v_bitop3_b32 v26, s13, 64, v1 bitop3:0x36\n"
    "v_bitop3_b32 v1, s13, v3, v1 bitop3:0x36\n"
    "s_cmp_lt_u32 s6, 64\n"
    "v_lshlrev_b32_e32 v3, 4, v4\n"
    "s_cbranch_scc1 .Lfd_R_FU_2\n"
    "v_lshlrev_b32_e32 v4, 4, v4\n"
    "s_mov_b64 s[2:3], 0\n"
    "s_branch .Lfd_R_FU_3\n"
    ".Lfd_R_FU_2:\n"
    "s_mov_b64 s[2:3], -1\n"
    ".Lfd_R_FU_3:\n"
    "s_load_dwordx4 s[8:11], s[0:1], 0x50\n"
    "s_add_i32 s7, s16, s4\n"
    "s_andn2_b64 vcc, exec, s[2:3]\n"
    "s_and_b32 s2, s5, 0x1e0\n"
    "s_cbranch_vccnz .Lfd_R_FU_5\n"
    "v_or_b32_e32 v4, s2, v29\n"
    "s_lshl_b32 s3, s7, 8\n"
    "v_lshl_or_b32 v4, v4, 13, s3\n"
    "s_movk_i32 s3, 0x80\n"
    "v_bitop3_b32 v8, v4, s3, v3 bitop3:0x36\n"
    "s_movk_i32 s3, 0xc0\n"
    "v_or_b32_e32 v5, v4, v3\n"
    "v_bitop3_b32 v6, v4, 64, v3 bitop3:0x36\n"
    "v_bitop3_b32 v4, v4, s3, v3 bitop3:0x36\n"
    "s_load_dwordx2 s[4:5], s[0:1], 0x18\n"
    "v_add_u32_e32 v7, 0x7c00, v6\n"
    "v_add_u32_e32 v9, 0xf800, v8\n"
    "v_add_u32_e32 v10, 0x17400, v4\n"
    "s_waitcnt lgkmcnt(0)\n"
    "s_add_u32 m0, s13, 0\n"
    "s_nop 0\n"
    "global_load_lds_dwordx4 v5, s[4:5] nt\n"
    "global_load_lds_dwordx4 v7, s[4:5] offset:1024 nt\n"
    "global_load_lds_dwordx4 v9, s[4:5] offset:2048 nt\n"
    "global_load_lds_dwordx4 v10, s[4:5] offset:3072 nt\n"
    "v_or_b32_e32 v5, 0x20000, v5\n"
    "v_add_u32_e32 v6, 0x27c00, v6\n"
    "v_add_u32_e32 v7, 0x2f800, v8\n"
    "v_add_u32_e32 v4, 0x37400, v4\n"
    "s_add_u32 m0, s13, 0x1000\n"
    "s_nop 0\n"
    "global_load_lds_dwordx4 v5, s[4:5] nt\n"
    "global_load_lds_dwordx4 v6, s[4:5] offset:1024 nt\n"
    "global_load_lds_dwordx4 v7, s[4:5] offset:2048 nt\n"
    "global_load_lds_dwordx4 v4, s[4:5] offset:3072 nt\n"
    "v_mov_b32_e32 v4, v3\n"
    ".Lfd_R_FU_5:\n"
    "s_load_dwordx2 s[4:5], s[0:1], 0x30\n"
    "s_lshl_b32 s0, s16, 8\n"
    "v_lshlrev_b32_e32 v3, 8, v30\n"
    "s_and_b32 s0, s0, 0x300\n"
    "v_and_b32_e32 v32, 48, v0\n"
    "s_waitcnt lgkmcnt(0)\n"
    "s_and_b32 s5, s5, 0xffff\n"
    "v_add3_u32 v3, v4, v3, s13\n"
    "v_or_b32_e32 v4, s0, v32\n"
    "s_cmp_gt_u32 s6, 63\n"
    "v_or_b32_e32 v24, 0x24000, v4\n"
    "v_and_b32_e32 v4, 1, v0\n"
    "s_cselect_b64 s[0:1], -1, 0\n"
    "s_lshl_b32 s3, s7, 11\n"
    "v_cmp_eq_u32_e32 vcc, 1, v4\n"
    "s_and_b32 s3, s3, 0xffffc000\n"
    "v_lshlrev_b32_e32 v4, 8, v0\n"
    "s_add_i32 s3, s3, 0x58000\n"
    "v_and_b32_e32 v4, 0x2000, v4\n"
    "v_or_b32_e32 v4, s3, v4\n"
    "s_lshl_b32 s3, s7, 10\n"
    "s_and_b32 s3, s3, 0x1c00\n"
    "v_and_or_b32 v5, v0, 31, s2\n"
    "v_mov_b32_e32 v33, 0x7ff00000\n"
    "s_or_b64 vcc, vcc, s[0:1]\n"
    "v_or_b32_e32 v4, s3, v4\n"
    "v_lshlrev_b32_e32 v5, 1, v5\n"
    "v_cndmask_b32_e32 v6, 0, v33, vcc\n"
    "v_or3_b32 v25, v5, v4, v6\n"
    "s_waitcnt vmcnt(0)\n"
    "ds_read_b128 v[4:7], v3\n"
    "ds_read_b128 v[8:11], v3 offset:4096\n"
    "v_xor_b32_e32 v16, 64, v3\n"
    "v_xor_b32_e32 v34, 0x80, v3\n"
    "v_xor_b32_e32 v3, 0xc0, v3\n"
    "ds_read_b128 v[12:15], v16\n"
    "ds_read_b128 v[16:19], v16 offset:4096\n"
    "ds_read_b128 v[20:23], v34\n"
    "ds_read_b128 v[34:37], v34 offset:4096\n"
    "ds_read_b128 v[38:41], v3\n"
    "ds_read_b128 v[42:45], v3 offset:4096\n"
    "s_waitcnt lgkmcnt(0)\n"
    "s_waitcnt lgkmcnt(0)\n"
    "s_mov_b32 s7, 0x20000\n"
    "s_mov_b32 s6, 0x600000\n"
    "s_mov_b32 s0, 0\n"
    "s_setprio 3\n"
    "s_waitcnt lgkmcnt(0)\n"
    "s_barrier\n"
    "ds_read_b128 v[46:49], v24\n"
    "ds_read_b128 v[50:53], v24 offset:64\n"
    "ds_read_b128 v[54:57], v24 offset:128\n"
    "ds_read_b128 v[58:61], v24 offset:192\n"
    "s_waitcnt lgkmcnt(3)\n"
    "v_mfma_f32_16x16x32_bf16 v[4:7], v[46:49], v[4:7], 0\n"
    "s_mov_b32 vcc_lo, 0xffff0000\n"
    "s_mov_b32 vcc_hi, vcc_lo\n"
    "v_mfma_f32_16x16x32_bf16 v[8:11], v[46:49], v[8:11], 0\n"
    "s_waitcnt lgkmcnt(2)\n"
    "v_mfma_f32_16x16x32_bf16 v[4:7], v[50:53], v[12:15], v[4:7]\n"
    "v_mfma_f32_16x16x32_bf16 v[8:11], v[50:53], v[16:19], v[8:11]\n"
    "s_waitcnt lgkmcnt(1)\n"
    "v_mfma_f32_16x16x32_bf16 v[4:7], v[54:57], v[20:23], v[4:7]\n"
    "v_mfma_f32_16x16x32_bf16 v[8:11], v[54:57], v[34:37], v[8:11]\n"
    "s_waitcnt lgkmcnt(0)\n"
    "v_mfma_f32_16x16x32_bf16 v[4:7], v[58:61], v[38:41], v[4:7]\n"
    "v_mfma_f32_16x16x32_bf16 v[6:9], v[58:61], v[42:45], v[8:11]\n"
    "s_nop 7\n"
    "v_cndmask_b32_e32 v3, v4, v6, vcc\n"
    "v_mul_f32_e32 v3, 0x3e0293ee, v3\n"
    "s_nop 1\n"
    "v_mov_b32_dpp v4, v3 quad_perm:[1,0,3,2] row_mask:0xf bank_mask:0xf bound_ctrl:1\n"
    "v_cvt_pk_bf16_f32 v3, v3, v4\n"
    "buffer_store_dword v3, v25, s[4:7], 0 offen sc1\n"
    "s_setprio 0\n"
    "s_barrier\n"
    "s_waitcnt lgkmcnt(0)\n"
    "s_mov_b32 s1, s94\n"
    "s_memrealtime s[2:3]\n"
    "s_waitcnt lgkmcnt(0)\n"
    "s_sub_i32 s2, s2, s1\n"
    "s_cmpk_gt_u32 s2, 0x1fd\n"
    "s_cbranch_scc1 .Lfd_R_FU_7\n"
    ".Lfd_R_FU_6:\n"
    "s_sleep 2\n"
    "s_memrealtime s[2:3]\n"
    "s_waitcnt lgkmcnt(0)\n"
    "s_sub_i32 s2, s2, s1\n"
    "s_cmpk_lt_u32 s2, 0x1fe\n"
    "s_cbranch_scc1 .Lfd_R_FU_6\n"
    ".Lfd_R_FU_7:\n"
    "s_mul_i32 s1, s14, 9\n"
    "v_lshlrev_b32_e32 v5, 4, v0\n"
    "s_add_i32 s1, s1, s15\n"
    "v_lshrrev_b32_e32 v3, 3, v2\n"
    "v_and_b32_e32 v5, 0x70, v5\n"
    "v_lshlrev_b32_e32 v34, 1, v2\n"
    "s_mul_i32 s3, s1, 0x12000\n"
    "v_mul_u32_u24_e32 v4, 0x480, v3\n"
    "v_bitop3_b32 v6, v34, v5, 32 bitop3:0x6c\n"
    "v_or3_b32 v4, s3, v4, v6\n"
    "v_mul_u32_u24_e32 v3, 0x2400, v3\n"
    "s_add_i32 s2, s12, -6\n"
    "v_and_b32_e32 v2, 32, v34\n"
    "v_lshl_add_u32 v4, s12, 7, v4\n"
    "v_lshl_add_u32 v3, s1, 7, v3\n"
    "v_xor_b32_e32 v6, 64, v4\n"
    "s_mul_i32 s3, s2, 0xd8000\n"
    "v_bitop3_b32 v2, v3, v2, v5 bitop3:0xf6\n"
    "v_add_u32_e32 v7, 0x2000, v6\n"
    "v_add_u32_e32 v8, 0x4000, v4\n"
    "v_add_u32_e32 v9, 0x6000, v6\n"
    "s_add_u32 m0, s13, 0x1000\n"
    "s_nop 0\n"
    "global_load_lds_dwordx4 v4, s[10:11] nt\n"
    "global_load_lds_dwordx4 v7, s[10:11] offset:1024 nt\n"
    "global_load_lds_dwordx4 v8, s[10:11] offset:2048 nt\n"
    "global_load_lds_dwordx4 v9, s[10:11] offset:3072 nt\n"
    "v_add_u32_e32 v35, s3, v2\n"
    "v_add_u32_e32 v7, 0x9000, v4\n"
    "v_add_u32_e32 v8, 0xb000, v6\n"
    "v_add_u32_e32 v4, 0xd000, v4\n"
    "v_add_u32_e32 v6, 0xf000, v6\n"
    "s_add_u32 m0, s13, 0x2000\n"
    "s_nop 0\n"
    "global_load_lds_dwordx4 v7, s[10:11] nt\n"
    "global_load_lds_dwordx4 v8, s[10:11] offset:1024 nt\n"
    "global_load_lds_dwordx4 v4, s[10:11] offset:2048 nt\n"
    "global_load_lds_dwordx4 v6, s[10:11] offset:3072 nt\n"
    "v_xor_b32_e32 v36, 64, v35\n"
    "v_add_u32_e32 v2, 0x11c00, v36\n"
    "s_barrier\n"
    "s_mul_i32 s3, s12, 3\n"
    "v_subrev_u32_e32 v18, s13, v28\n"
    "s_mul_i32 s10, s12, 0x60\n"
    "v_mov_b32_e32 v19, 0x60\n"
    "v_add_u32_e32 v3, 0x23800, v35\n"
    "v_add_u32_e32 v4, 0x35400, v36\n"
    "s_add_u32 m0, s13, 0\n"
    "s_nop 0\n"
    "global_load_lds_dwordx4 v35, s[8:9] nt\n"
    "global_load_lds_dwordx4 v2, s[8:9] offset:1024 nt\n"
    "global_load_lds_dwordx4 v3, s[8:9] offset:2048 nt\n"
    "global_load_lds_dwordx4 v4, s[8:9] offset:3072 nt\n"
    "v_bitop3_b32 v2, s10, v18, v19 bitop3:0x6c\n"
    "s_lshr_b32 s10, s3, 2\n"
    "s_mulk_i32 s10, 0x3000\n"
    "v_add_u32_e32 v8, s10, v2\n"
    "s_add_i32 s10, s3, 1\n"
    "s_lshl_b32 s11, s10, 5\n"
    "s_lshr_b32 s10, s10, 2\n"
    "v_bitop3_b32 v10, s11, v18, v19 bitop3:0x6c\n"
    "s_mulk_i32 s10, 0x3000\n"
    "v_add_u32_e32 v16, s10, v10\n"
    "s_add_i32 s10, s3, 2\n"
    "s_lshl_b32 s11, s10, 5\n"
    "s_lshr_b32 s10, s10, 2\n"
    "s_waitcnt vmcnt(4)\n"
    "v_bitop3_b32 v18, s11, v18, v19 bitop3:0x6c\n"
    "s_mulk_i32 s10, 0x3000\n"
    "s_barrier\n"
    "v_add_u32_e32 v20, s10, v18\n"
    "ds_read_b64_tr_b16 v[2:3], v8 offset:4096\n"
    "ds_read_b64_tr_b16 v[4:5], v8 offset:4608\n"
    "ds_read_b64_tr_b16 v[6:7], v8 offset:8192\n"
    "ds_read_b64_tr_b16 v[8:9], v8 offset:8704\n"
    "ds_read_b64_tr_b16 v[10:11], v16 offset:4096\n"
    "ds_read_b64_tr_b16 v[12:13], v16 offset:4608\n"
    "ds_read_b64_tr_b16 v[14:15], v16 offset:8192\n"
    "ds_read_b64_tr_b16 v[16:17], v16 offset:8704\n"
    "ds_read_b64_tr_b16 v[22:23], v20 offset:4096\n"
    "ds_read_b64_tr_b16 v[24:25], v20 offset:4608\n"
    "ds_read_b64_tr_b16 v[18:19], v20 offset:8192\n"
    "ds_read_b64_tr_b16 v[20:21], v20 offset:8704\n"
    "s_waitcnt lgkmcnt(0)\n"
    "s_waitcnt lgkmcnt(0)\n"
    "s_barrier\n"
    "v_and_b32_e32 v0, 3, v0\n"
    "v_add_u32_e32 v37, 0x48000, v35\n"
    "v_add_u32_e32 v38, 0x59c00, v36\n"
    "v_add_u32_e32 v39, 0x6b800, v35\n"
    "v_add_u32_e32 v40, 0x7d400, v36\n"
    "s_add_u32 m0, s13, 0x1000\n"
    "s_nop 0\n"
    "global_load_lds_dwordx4 v37, s[8:9] nt\n"
    "global_load_lds_dwordx4 v38, s[8:9] offset:1024 nt\n"
    "global_load_lds_dwordx4 v39, s[8:9] offset:2048 nt\n"
    "global_load_lds_dwordx4 v40, s[8:9] offset:3072 nt\n"
    "v_min_u32_e32 v0, 2, v0\n"
    "v_add_u32_e32 v37, 0x90000, v35\n"
    "v_add_u32_e32 v35, 0xb3800, v35\n"
    "s_barrier\n"
    "v_mul_u32_u24_e32 v0, 0xd0, v0\n"
    "s_mulk_i32 s12, 0x280\n"
    "v_add_u32_e32 v38, 0xa1c00, v36\n"
    "v_add_u32_e32 v36, 0xc5400, v36\n"
    "s_add_u32 m0, s13, 0x2000\n"
    "s_nop 0\n"
    "global_load_lds_dwordx4 v37, s[8:9] nt\n"
    "global_load_lds_dwordx4 v38, s[8:9] offset:1024 nt\n"
    "global_load_lds_dwordx4 v35, s[8:9] offset:2048 nt\n"
    "global_load_lds_dwordx4 v36, s[8:9] offset:3072 nt\n"
    "v_add3_u32 v0, v0, v32, s12\n"
    "v_lshl_or_b32 v35, s2, 9, v31\n"
    "v_add_u32_e32 v0, 0x23f00, v0\n"
    "v_mov_b32_e32 v98, 0x27800\n"
    "v_add_u32_e32 v102, 0x26c04, v35\n"
    "v_or_b32_e32 v31, 0x26c00, v31\n"
    "v_or_b32_e32 v103, 0x25d00, v32\n"
    "v_or_b32_e32 v31, 0x25d00, v34\n"
    "v_add_u32_e32 v32, s3, v29\n"
    "v_mov_b32_e32 v31, 0x27fd4\n"
    "ds_read_b32 v31, v31\n"
    "v_lshlrev_b32_e32 v34, 14, v32\n"
    "v_add_u32_e32 v35, 0xfffba000, v34\n"
    "v_cmp_lt_u32_e64 s[2:3], 17, v32\n"
    "v_lshlrev_b32_e32 v30, 2, v30\n"
    "v_lshl_or_b32 v30, s1, 6, v30\n"
    "v_cndmask_b32_e64 v32, v34, v35, s[2:3]\n"
    "s_mov_b32 s1, 0x10000\n"
    "v_cmp_eq_u32_e64 s[2:3], 3, v29\n"
    "v_add3_u32 v30, v30, v32, s1\n"
    "s_nop 0\n"
    "v_cndmask_b32_e64 v29, 0, v33, s[2:3]\n"
    "s_waitcnt lgkmcnt(0)\n"
    "v_bitop3_b32 v29, v31, v29, v30 bitop3:0xde\n"
    "s_waitcnt vmcnt(4)\n"
    "ds_read_b64_tr_b16 v[30:31], v28\n"
    "ds_read_b64_tr_b16 v[32:33], v28 offset:512\n"
    "ds_read_b64_tr_b16 v[34:35], v28 offset:4096\n"
    "ds_read_b64_tr_b16 v[36:37], v28 offset:4608\n"
    "ds_read_b64_tr_b16 v[38:39], v27\n"
    "ds_read_b64_tr_b16 v[40:41], v27 offset:512\n"
    "ds_read_b64_tr_b16 v[42:43], v27 offset:4096\n"
    "ds_read_b64_tr_b16 v[44:45], v27 offset:4608\n"
    "ds_read_b64_tr_b16 v[46:47], v26\n"
    "ds_read_b64_tr_b16 v[48:49], v26 offset:512\n"
    "ds_read_b64_tr_b16 v[50:51], v26 offset:4096\n"
    "ds_read_b64_tr_b16 v[52:53], v26 offset:4608\n"
    "ds_read_b64_tr_b16 v[54:55], v1\n"
    "ds_read_b64_tr_b16 v[56:57], v1 offset:512\n"
    "ds_read_b64_tr_b16 v[58:59], v1 offset:4096\n"
    "ds_read_b64_tr_b16 v[60:61], v1 offset:4608\n"
    "s_waitcnt vmcnt(0)\n"
    "ds_read_b64_tr_b16 v[62:63], v28 offset:8192\n"
    "ds_read_b64_tr_b16 v[64:65], v28 offset:8704\n"
    "ds_read_b64_tr_b16 v[66:67], v27 offset:8192\n"
    "ds_read_b64_tr_b16 v[68:69], v27 offset:8704\n"
    "ds_read_b64_tr_b16 v[70:71], v26 offset:8192\n"
    "ds_read_b64_tr_b16 v[72:73], v26 offset:8704\n"
    "ds_read_b64_tr_b16 v[74:75], v1 offset:8192\n"
    "ds_read_b64_tr_b16 v[76:77], v1 offset:8704\n"
    "s_waitcnt lgkmcnt(0)\n"
    "s_barrier\n"
    "ds_read_b128 v[78:81], v0\n"
    "ds_read_b128 v[82:85], v0 offset:64\n"
    "ds_read_b128 v[86:89], v0 offset:128\n"
    "ds_read_b128 v[90:93], v98\n"
    "ds_read_b128 v[94:97], v98 offset:16\n"
    "ds_read_b128 v[98:101], v98 offset:32\n"
    "s_waitcnt lgkmcnt(5)\n"
    "v_mfma_f32_16x16x32_bf16 v[30:33], v[78:81], v[30:33], 0\n"
    "s_waitcnt lgkmcnt(4)\n"
    "v_mfma_f32_16x16x32_bf16 v[30:33], v[82:85], v[34:37], v[30:33]\n"
    "s_waitcnt lgkmcnt(3)\n"
    "v_mfma_f32_16x16x32_bf16 v[30:33], v[86:89], v[62:65], v[30:33]\n"
    "s_waitcnt lgkmcnt(2)\n"
    "v_add_f32_e32 v0, v90, v92\n"
    "s_waitcnt lgkmcnt(1)\n"
    "v_add_f32_e32 v1, v94, v96\n"
    "v_mfma_f32_16x16x32_bf16 v[34:37], v[78:81], v[38:41], 0\n"
    "v_add_f32_e32 v0, v0, v1\n"
    "s_waitcnt lgkmcnt(0)\n"
    "v_add_f32_e32 v26, v98, v100\n"
    "v_mfma_f32_16x16x32_bf16 v[34:37], v[82:85], v[42:45], v[34:37]\n"
    "v_add_f32_e32 v1, v91, v93\n"
    "v_add_f32_e32 v27, v95, v97\n"
    "v_mfma_f32_16x16x32_bf16 v[34:37], v[86:89], v[66:69], v[34:37]\n"
    "v_add_f32_e32 v0, v0, v26\n"
    "v_add_f32_e32 v28, v99, v101\n"
    "v_mfma_f32_16x16x32_bf16 v[38:41], v[78:81], v[46:49], 0\n"
    "v_add_f32_e32 v1, v1, v27\n"
    "v_fma_f32 v26, -v0, v0, v28\n"
    "v_mfma_f32_16x16x32_bf16 v[38:41], v[82:85], v[50:53], v[38:41]\n"
    "v_add_f32_e32 v1, v1, v26\n"
    "v_maximum3_f32 v1, v1, 0, 0\n"
    "v_mfma_f32_16x16x32_bf16 v[38:41], v[86:89], v[70:73], v[38:41]\n"
    "v_add_f32_e32 v1, 0x3727c5ac, v1\n"
    "v_rsq_f32_e32 v1, v1\n"
    "v_mfma_f32_16x16x32_bf16 v[42:45], v[78:81], v[54:57], 0\n"
    "v_fma_f32 v26, -v0, v31, v30\n"
    "v_fmac_f32_e32 v32, v1, v26\n"
    "v_mfma_f32_16x16x32_bf16 v[42:45], v[82:85], v[58:61], v[42:45]\n"
    "v_fma_f32 v26, -v0, v35, v34\n"
    "v_fmac_f32_e32 v36, v1, v26\n"
    "v_mfma_f32_16x16x32_bf16 v[42:45], v[86:89], v[74:77], v[42:45]\n"
    "v_cndmask_b32_e32 v26, v32, v36, vcc\n"
    "v_fma_f32 v27, -v0, v39, v38\n"
    "v_fmac_f32_e32 v40, v1, v27\n"
    "s_nop 4\n"
    "v_fma_f32 v0, -v0, v43, v42\n"
    "v_fmac_f32_e32 v44, v1, v0\n"
    "v_cndmask_b32_e32 v0, v40, v44, vcc\n"
    "s_mov_b32 s1, -1\n"
    "v_cndmask_b32_e64 v0, v26, v0, s[0:1]\n"
    "ds_write_b32 v102, v0\n"
    "s_waitcnt lgkmcnt(0)\n"
    "s_barrier\n"
    "s_waitcnt lgkmcnt(0)\n"
    "s_barrier\n"
    "ds_read_b128 v[30:33], v103\n"
    "ds_read_b128 v[34:37], v103 offset:64\n"
    "s_xor_b64 s[2:3], s[0:1], -1\n"
    "s_waitcnt lgkmcnt(1)\n"
    "v_mfma_f32_16x16x32_bf16 v[0:3], v[30:33], v[2:5], 0\n"
    "s_waitcnt lgkmcnt(0)\n"
    "v_mfma_f32_16x16x32_bf16 v[0:3], v[34:37], v[6:9], v[0:3]\n"
    "v_mfma_f32_16x16x32_bf16 v[2:5], v[30:33], v[10:13], 0\n"
    "v_mfma_f32_16x16x32_bf16 v[2:5], v[34:37], v[14:17], v[2:5]\n"
    "v_mfma_f32_16x16x32_bf16 v[4:7], v[30:33], v[22:25], 0\n"
    "v_mfma_f32_16x16x32_bf16 v[4:7], v[34:37], v[18:21], v[4:7]\n"
    "s_and_saveexec_b64 s[0:1], s[2:3]\n"
    "s_mov_b32 vcc_lo, 0xffff0000\n"
    "s_mov_b32 vcc_hi, vcc_lo\n"
    "s_nop 4\n"
    "v_cndmask_b32_e32 v4, v0, v2, vcc\n"
    "s_or_b64 exec, exec, s[0:1]\n"
    "buffer_store_dword v4, v29, s[4:7], 0 offen sc1\n"
    "s_endpgm\n"
    ".p2align 7\n"
    ".Lfd_R_FV:\n"
    "v_and_b32_e32 v14, 63, v0\n"
    "v_bfe_u32 v29, v0, 4, 2\n"
    "v_bfe_u32 v1, v0, 2, 4\n"
    "v_lshlrev_b32_e32 v3, 5, v0\n"
    "v_lshlrev_b32_e32 v30, 3, v14\n"
    "v_readfirstlane_b32 s3, v0\n"
    "v_lshlrev_b32_e32 v2, 10, v29\n"
    "v_and_b32_e32 v3, 0x180, v3\n"
    "v_and_b32_e32 v4, 8, v30\n"
    "v_bfe_u32 v5, v0, 1, 1\n"
    "v_and_b32_e32 v6, 2, v1\n"
    "v_and_b32_e32 v1, 4, v1\n"
    "s_lshr_b32 s3, s3, 6\n"
    "v_or3_b32 v1, v5, v6, v1\n"
    "v_or3_b32 v2, v2, v4, v3\n"
    "s_mul_i32 s12, s3, 0x3000\n"
    "v_lshl_or_b32 v1, v1, 4, v2\n"
    "v_mov_b32_e32 v2, 0x60\n"
    "v_or_b32_e32 v28, s12, v1\n"
    "v_bitop3_b32 v27, s12, 32, v1 bitop3:0x36\n"
    "v_bitop3_b32 v26, s12, 64, v1 bitop3:0x36\n"
    "v_bitop3_b32 v1, s12, v2, v1 bitop3:0x36\n"
    "s_waitcnt vmcnt(0)\n"
    "v_mov_b32 v2, v162\n"
    "v_mov_b32 v4, v163\n"
    "s_waitcnt lgkmcnt(0)\n"
    "s_mov_b32 s4, s94\n"
    "s_memrealtime s[6:7]\n"
    "s_load_dwordx4 s[8:11], s[0:1], 0x50\n"
    "s_waitcnt lgkmcnt(0)\n"
    "s_sub_i32 s5, s6, s4\n"
    "s_cmpk_gt_u32 s5, 0x103\n"
    "s_cbranch_scc1 .Lfd_R_FV_2\n"
    ".Lfd_R_FV_1:\n"
    "s_sleep 2\n"
    "s_memrealtime s[6:7]\n"
    "s_waitcnt lgkmcnt(0)\n"
    "s_sub_i32 s5, s6, s4\n"
    "s_cmpk_lt_u32 s5, 0x104\n"
    "s_cbranch_scc1 .Lfd_R_FV_1\n"
    ".Lfd_R_FV_2:\n"
    "s_load_dwordx2 s[4:5], s[0:1], 0x30\n"
    "v_mov_b32_e32 v3, 0\n"
    "v_lshl_add_u64 v[2:3], v[2:3], 0, 1\n"
    "v_mov_b32_e32 v5, 0x2000\n"
    "s_mov_b32 s7, 0x20000\n"
    "s_mov_b32 s6, 0x600000\n"
    "s_waitcnt lgkmcnt(0)\n"
    "s_and_b32 s5, s5, 0xffff\n"
    "v_lshl_or_b32 v6, v14, 7, v5\n"
    "v_add_u32_e32 v3, v4, v3\n"
    "s_mov_b32 s13, 0x8000\n"
    "s_brev_b32 s14, -2\n"
    "s_branch .Lfd_R_FV_4\n"
    ".Lfd_R_FV_3:\n"
    "s_andn2_b64 vcc, exec, s[0:1]\n"
    "s_cbranch_vccz .Lfd_R_FV_6\n"
    ".Lfd_R_FV_4:\n"
    "buffer_load_dwordx2 v[4:5], v6, s[4:7], 0 offen sc1\n"
    "s_waitcnt vmcnt(0)\n"
    "v_cmp_eq_u32_e32 vcc, v4, v2\n"
    "v_bitop3_b32 v4, v5, s14, v3 bitop3:0x48\n"
    "v_cmp_eq_u32_e64 s[0:1], 0, v4\n"
    "s_and_b64 s[0:1], vcc, s[0:1]\n"
    "s_nop 0\n"
    "v_cndmask_b32_e64 v4, 0, 1, s[0:1]\n"
    "v_cmp_ne_u32_e32 vcc, 0, v4\n"
    "s_cmp_eq_u64 vcc, exec\n"
    "s_mov_b64 s[0:1], -1\n"
    "s_cbranch_scc1 .Lfd_R_FV_3\n"
    "s_add_i32 s13, s13, -1\n"
    "s_cmp_eq_u32 s13, 0\n"
    "s_cselect_b64 s[0:1], -1, 0\n"
    "s_sleep 1\n"
    "s_branch .Lfd_R_FV_3\n"
    ".Lfd_R_FV_6:\n"
    "v_cmp_gt_i32_e32 vcc, 0, v5\n"
    "s_and_b32 s15, vcc_hi, 0xc00000\n"
    "s_and_b32 s14, vcc_lo, 0xc00000c0\n"
    "s_mov_b32 s1, 0\n"
    "s_bcnt1_i32_b64 s0, s[14:15]\n"
    "v_cmp_gt_u64_e64 s[14:15], s[0:1], 3\n"
    "v_and_b32_e32 v2, 31, v0\n"
    "v_lshrrev_b32_e32 v3, 5, v14\n"
    "v_cndmask_b32_e64 v4, 0, 1, s[14:15]\n"
    "s_and_b32 s15, vcc_hi, 0xc00000c0\n"
    "s_and_b32 s14, vcc_lo, 0xc000\n"
    "s_bcnt1_i32_b64 s0, s[14:15]\n"
    "v_cmp_gt_u64_e64 s[14:15], s[0:1], 3\n"
    "s_and_b64 s[14:15], s[14:15], exec\n"
    "s_cselect_b32 s0, 2, 0\n"
    "s_and_b32 s15, vcc_hi, 0xc000\n"
    "s_and_b32 s14, vcc_lo, 0xc00000\n"
    "v_or_b32_e32 v4, s0, v4\n"
    "s_bcnt1_i32_b64 s0, s[14:15]\n"
    "v_cmp_gt_u64_e64 s[0:1], s[0:1], 2\n"
    "s_and_b64 s[0:1], s[0:1], exec\n"
    "s_cselect_b32 s0, 4, 0\n"
    "v_or_b32_e32 v4, s0, v4\n"
    "s_lshl_b32 s13, s2, 11\n"
    "v_readfirstlane_b32 s0, v4\n"
    "s_lshl_b32 s0, s0, 13\n"
    "s_and_b32 s1, s0, 0x2000\n"
    "v_mov_b32_e32 v5, s1\n"
    "s_lshl_b32 s1, s2, 2\n"
    "s_and_b32 s1, s1, 12\n"
    "v_mov_b32_e32 v4, 0x27fd4\n"
    "s_add_i32 s1, s3, s1\n"
    "s_and_b32 s13, s13, 0x2000\n"
    "ds_write_b32 v4, v5\n"
    "s_lshl_b32 s1, s1, 14\n"
    "v_lshlrev_b32_e32 v4, 9, v3\n"
    "v_lshl_or_b32 v5, v2, 4, s13\n"
    "v_or3_b32 v4, v4, v5, s1\n"
    "v_add_u32_e32 v4, 0xffff1200, v4\n"
    "v_mov_b32_e32 v5, 0x2000\n"
    "v_bitop3_b32 v16, s0, v4, v5 bitop3:0x6c\n"
    "s_lshl_b32 s0, s3, 8\n"
    "s_add_i32 s0, s0, 0x23800\n"
    "v_lshlrev_b32_e32 v2, 3, v2\n"
    "v_lshlrev_b32_e32 v3, 2, v3\n"
    "v_or3_b32 v15, s0, v2, v3\n"
    "s_setprio 3\n"
    "s_waitcnt lgkmcnt(0)\n"
    "s_mov_b32 s0, s94\n"
    "s_memrealtime s[14:15]\n"
    "s_waitcnt lgkmcnt(0)\n"
    "s_sub_i32 s1, s14, s0\n"
    "s_cmpk_gt_u32 s1, 0x185\n"
    "s_cbranch_scc1 .Lfd_R_FV_8\n"
    ".Lfd_R_FV_7:\n"
    "s_sleep 2\n"
    "s_memrealtime s[14:15]\n"
    "s_waitcnt lgkmcnt(0)\n"
    "s_sub_i32 s1, s14, s0\n"
    "s_cmpk_lt_u32 s1, 0x186\n"
    "s_cbranch_scc1 .Lfd_R_FV_7\n"
    ".Lfd_R_FV_8:\n"
    "s_and_b32 s13, s2, 7\n"
    "v_add_u32_e32 v17, 0x400, v16\n"
    "v_add_u32_e32 v18, 0x800, v16\n"
    "v_mov_b32_e32 v19, 0x7fff\n"
    ".Lfd_R_FV_9:\n"
    "buffer_load_dwordx4 v[6:9], v16, s[4:7], 0 offen sc1\n"
    "buffer_load_dwordx4 v[10:13], v17, s[4:7], 0 offen sc1\n"
    "buffer_load_dwordx4 v[2:5], v18, s[4:7], 0 offen sc1\n"
    "v_subrev_co_u32_e32 v19, vcc, 1, v19\n"
    "s_waitcnt vmcnt(2)\n"
    "v_max_u32_e32 v20, v6, v7\n"
    "v_max3_u32 v20, v20, v8, v9\n"
    "s_waitcnt vmcnt(1)\n"
    "v_max3_u32 v20, v20, v10, v11\n"
    "v_max3_u32 v20, v20, v12, v13\n"
    "s_waitcnt vmcnt(0)\n"
    "v_max3_u32 v20, v20, v2, v3\n"
    "v_max3_u32 v20, v20, v4, v5\n"
    "v_cmp_ne_u32_e64 s[0:1], -1, v20\n"
    "s_cmp_eq_u64 s[0:1], exec\n"
    "s_cselect_b64 s[0:1], -1, 0\n"
    "s_or_b64 s[0:1], s[0:1], vcc\n"
    "s_andn2_b64 vcc, exec, s[0:1]\n"
    "s_cbranch_vccnz .Lfd_R_FV_9\n"
    "v_pk_add_f32 v[8:9], v[8:9], v[12:13]\n"
    "v_pk_add_f32 v[6:7], v[6:7], v[10:11]\n"
    "v_pk_add_f32 v[4:5], v[8:9], v[4:5]\n"
    "v_pk_add_f32 v[2:3], v[6:7], v[2:3]\n"
    "s_nop 1\n"
    "v_permlane32_swap_b32_e32 v2, v4\n"
    "v_permlane32_swap_b32_e32 v3, v5\n"
    "v_add_f32_e32 v2, v2, v4\n"
    "v_add_f32_e32 v3, v3, v5\n"
    "v_cvt_pk_bf16_f32 v2, v2, v3\n"
    "ds_write_b32 v15, v2\n"
    "s_waitcnt lgkmcnt(0)\n"
    "s_barrier\n"
    "s_setprio 0\n"
    "s_barrier\n"
    "s_waitcnt lgkmcnt(0)\n"
    "s_mov_b32 s0, s94\n"
    "s_memrealtime s[14:15]\n"
    "s_waitcnt lgkmcnt(0)\n"
    "s_sub_i32 s1, s14, s0\n"
    "s_cmpk_gt_u32 s1, 0x1fd\n"
    "s_cbranch_scc1 .Lfd_R_FV_12\n"
    ".Lfd_R_FV_11:\n"
    "s_sleep 2\n"
    "s_memrealtime s[14:15]\n"
    "s_waitcnt lgkmcnt(0)\n"
    "s_sub_i32 s1, s14, s0\n"
    "s_cmpk_lt_u32 s1, 0x1fe\n"
    "s_cbranch_scc1 .Lfd_R_FV_11\n"
    ".Lfd_R_FV_12:\n"
    "s_lshr_b32 s0, s2, 3\n"
    "s_mul_i32 s1, s13, 9\n"
    "v_lshlrev_b32_e32 v3, 4, v0\n"
    "s_add_i32 s0, s1, s0\n"
    "v_and_b32_e32 v3, 0x70, v3\n"
    "v_lshlrev_b32_e32 v31, 1, v14\n"
    "v_lshrrev_b32_e32 v2, 3, v14\n"
    "s_cmp_lg_u32 s3, 8\n"
    "v_bitop3_b32 v3, v31, v3, 32 bitop3:0x6c\n"
    "s_cbranch_scc1 .Lfd_R_FV_14\n"
    "s_mul_i32 s1, s0, 0x12000\n"
    "v_mul_u32_u24_e32 v4, 0x480, v2\n"
    "v_or3_b32 v4, s1, v4, v3\n"
    "v_lshl_add_u32 v4, s3, 7, v4\n"
    "v_xor_b32_e32 v5, 64, v4\n"
    "v_add_u32_e32 v6, 0x2000, v5\n"
    "v_add_u32_e32 v7, 0x4000, v4\n"
    "v_add_u32_e32 v8, 0x6000, v5\n"
    "s_add_u32 m0, s12, 0x1000\n"
    "s_nop 0\n"
    "global_load_lds_dwordx4 v4, s[10:11] nt\n"
    "global_load_lds_dwordx4 v6, s[10:11] offset:1024 nt\n"
    "global_load_lds_dwordx4 v7, s[10:11] offset:2048 nt\n"
    "global_load_lds_dwordx4 v8, s[10:11] offset:3072 nt\n"
    "v_add_u32_e32 v9, 0x9000, v4\n"
    "v_add_u32_e32 v10, 0xb000, v5\n"
    "v_add_u32_e32 v11, 0xd000, v4\n"
    "v_add_u32_e32 v5, 0xf000, v5\n"
    "s_add_u32 m0, s12, 0x2000\n"
    "s_nop 0\n"
    "global_load_lds_dwordx4 v9, s[10:11] nt\n"
    "global_load_lds_dwordx4 v10, s[10:11] offset:1024 nt\n"
    "global_load_lds_dwordx4 v11, s[10:11] offset:2048 nt\n"
    "global_load_lds_dwordx4 v5, s[10:11] offset:3072 nt\n"
    ".Lfd_R_FV_14:\n"
    "v_mul_u32_u24_e32 v2, 0x2400, v2\n"
    "s_add_i32 s1, s3, -6\n"
    "v_lshl_add_u32 v2, s0, 7, v2\n"
    "s_mul_i32 s2, s1, 0xd8000\n"
    "v_or_b32_e32 v2, v2, v3\n"
    "v_add_u32_e32 v32, s2, v2\n"
    "v_xor_b32_e32 v33, 64, v32\n"
    "v_add_u32_e32 v2, 0x11c00, v33\n"
    "s_barrier\n"
    "s_mul_i32 s2, s3, 3\n"
    "v_subrev_u32_e32 v18, s12, v28\n"
    "s_mul_i32 s10, s3, 0x60\n"
    "v_mov_b32_e32 v19, 0x60\n"
    "v_add_u32_e32 v3, 0x23800, v32\n"
    "v_add_u32_e32 v4, 0x35400, v33\n"
    "s_add_u32 m0, s12, 0\n"
    "s_nop 0\n"
    "global_load_lds_dwordx4 v32, s[8:9] nt\n"
    "global_load_lds_dwordx4 v2, s[8:9] offset:1024 nt\n"
    "global_load_lds_dwordx4 v3, s[8:9] offset:2048 nt\n"
    "global_load_lds_dwordx4 v4, s[8:9] offset:3072 nt\n"
    "v_bitop3_b32 v2, s10, v18, v19 bitop3:0x6c\n"
    "s_lshr_b32 s10, s2, 2\n"
    "s_mulk_i32 s10, 0x3000\n"
    "v_add_u32_e32 v8, s10, v2\n"
    "s_add_i32 s10, s2, 1\n"
    "s_lshl_b32 s11, s10, 5\n"
    "s_lshr_b32 s10, s10, 2\n"
    "v_bitop3_b32 v10, s11, v18, v19 bitop3:0x6c\n"
    "s_mulk_i32 s10, 0x3000\n"
    "v_add_u32_e32 v16, s10, v10\n"
    "s_add_i32 s10, s2, 2\n"
    "s_lshl_b32 s11, s10, 5\n"
    "s_lshr_b32 s10, s10, 2\n"
    "s_waitcnt vmcnt(4)\n"
    "v_bitop3_b32 v18, s11, v18, v19 bitop3:0x6c\n"
    "s_mulk_i32 s10, 0x3000\n"
    "s_barrier\n"
    "v_add_u32_e32 v20, s10, v18\n"
    "ds_read_b64_tr_b16 v[2:3], v8 offset:4096\n"
    "ds_read_b64_tr_b16 v[4:5], v8 offset:4608\n"
    "ds_read_b64_tr_b16 v[6:7], v8 offset:8192\n"
    "ds_read_b64_tr_b16 v[8:9], v8 offset:8704\n"
    "ds_read_b64_tr_b16 v[10:11], v16 offset:4096\n"
    "ds_read_b64_tr_b16 v[12:13], v16 offset:4608\n"
    "ds_read_b64_tr_b16 v[14:15], v16 offset:8192\n"
    "ds_read_b64_tr_b16 v[16:17], v16 offset:8704\n"
    "ds_read_b64_tr_b16 v[22:23], v20 offset:4096\n"
    "ds_read_b64_tr_b16 v[24:25], v20 offset:4608\n"
    "ds_read_b64_tr_b16 v[18:19], v20 offset:8192\n"
    "ds_read_b64_tr_b16 v[20:21], v20 offset:8704\n"
    "s_waitcnt lgkmcnt(0)\n"
    "s_waitcnt lgkmcnt(0)\n"
    "s_barrier\n"
    "v_add_u32_e32 v34, 0x48000, v32\n"
    "v_add_u32_e32 v35, 0x59c00, v33\n"
    "v_add_u32_e32 v36, 0x6b800, v32\n"
    "v_add_u32_e32 v37, 0x7d400, v33\n"
    "s_add_u32 m0, s12, 0x1000\n"
    "s_nop 0\n"
    "global_load_lds_dwordx4 v34, s[8:9] nt\n"
    "global_load_lds_dwordx4 v35, s[8:9] offset:1024 nt\n"
    "global_load_lds_dwordx4 v36, s[8:9] offset:2048 nt\n"
    "global_load_lds_dwordx4 v37, s[8:9] offset:3072 nt\n"
    "v_add_u32_e32 v34, 0x90000, v32\n"
    "v_add_u32_e32 v32, 0xb3800, v32\n"
    "s_barrier\n"
    "v_add_u32_e32 v35, 0xa1c00, v33\n"
    "v_add_u32_e32 v33, 0xc5400, v33\n"
    "s_add_u32 m0, s12, 0x2000\n"
    "s_nop 0\n"
    "global_load_lds_dwordx4 v34, s[8:9] nt\n"
    "global_load_lds_dwordx4 v35, s[8:9] offset:1024 nt\n"
    "global_load_lds_dwordx4 v32, s[8:9] offset:2048 nt\n"
    "global_load_lds_dwordx4 v33, s[8:9] offset:3072 nt\n"
    "v_and_b32_e32 v32, 3, v0\n"
    "v_min_u32_e32 v32, 2, v32\n"
    "v_mul_u32_u24_e32 v32, 0xd0, v32\n"
    "v_and_b32_e32 v33, 48, v0\n"
    "s_mulk_i32 s3, 0x280\n"
    "v_add3_u32 v32, v32, v33, s3\n"
    "v_add_u32_e32 v86, 0x23f00, v32\n"
    "v_lshl_or_b32 v32, s1, 9, v30\n"
    "v_mov_b32_e32 v98, 0x27800\n"
    "v_add_u32_e32 v102, 0x26c04, v32\n"
    "v_or_b32_e32 v30, 0x26c00, v30\n"
    "v_or_b32_e32 v103, 0x25d00, v33\n"
    "v_or_b32_e32 v30, 0x25d00, v31\n"
    "v_add_u32_e32 v31, s2, v29\n"
    "v_mov_b32_e32 v30, 0x27fd4\n"
    "ds_read_b32 v30, v30\n"
    "v_lshlrev_b32_e32 v32, 14, v31\n"
    "v_lshlrev_b32_e32 v0, 2, v0\n"
    "v_add_u32_e32 v33, 0xfffba000, v32\n"
    "v_cmp_lt_u32_e32 vcc, 17, v31\n"
    "v_and_b32_e32 v0, 60, v0\n"
    "v_lshl_or_b32 v0, s0, 6, v0\n"
    "v_cndmask_b32_e32 v31, v32, v33, vcc\n"
    "s_mov_b32 s0, 0x10000\n"
    "v_add3_u32 v0, v0, v31, s0\n"
    "v_mov_b32_e32 v31, 0x7ff00000\n"
    "v_cmp_eq_u32_e32 vcc, 3, v29\n"
    "s_mov_b32 s0, 0\n"
    "s_mov_b32 s1, -1\n"
    "v_cndmask_b32_e32 v29, 0, v31, vcc\n"
    "s_waitcnt lgkmcnt(0)\n"
    "v_bitop3_b32 v29, v30, v29, v0 bitop3:0xde\n"
    "s_waitcnt vmcnt(4)\n"
    "ds_read_b64_tr_b16 v[30:31], v28\n"
    "ds_read_b64_tr_b16 v[32:33], v28 offset:512\n"
    "ds_read_b64_tr_b16 v[34:35], v28 offset:4096\n"
    "ds_read_b64_tr_b16 v[36:37], v28 offset:4608\n"
    "ds_read_b64_tr_b16 v[38:39], v27\n"
    "ds_read_b64_tr_b16 v[40:41], v27 offset:512\n"
    "ds_read_b64_tr_b16 v[42:43], v27 offset:4096\n"
    "ds_read_b64_tr_b16 v[44:45], v27 offset:4608\n"
    "ds_read_b64_tr_b16 v[46:47], v26\n"
    "ds_read_b64_tr_b16 v[48:49], v26 offset:512\n"
    "ds_read_b64_tr_b16 v[50:51], v26 offset:4096\n"
    "ds_read_b64_tr_b16 v[52:53], v26 offset:4608\n"
    "ds_read_b64_tr_b16 v[54:55], v1\n"
    "ds_read_b64_tr_b16 v[56:57], v1 offset:512\n"
    "ds_read_b64_tr_b16 v[58:59], v1 offset:4096\n"
    "ds_read_b64_tr_b16 v[60:61], v1 offset:4608\n"
    "s_waitcnt vmcnt(0)\n"
    "ds_read_b64_tr_b16 v[62:63], v28 offset:8192\n"
    "ds_read_b64_tr_b16 v[64:65], v28 offset:8704\n"
    "ds_read_b64_tr_b16 v[66:67], v27 offset:8192\n"
    "ds_read_b64_tr_b16 v[68:69], v27 offset:8704\n"
    "ds_read_b64_tr_b16 v[70:71], v26 offset:8192\n"
    "ds_read_b64_tr_b16 v[72:73], v26 offset:8704\n"
    "ds_read_b64_tr_b16 v[74:75], v1 offset:8192\n"
    "ds_read_b64_tr_b16 v[76:77], v1 offset:8704\n"
    "s_waitcnt lgkmcnt(0)\n"
    "s_barrier\n"
    "ds_read_b128 v[78:81], v86\n"
    "ds_read_b128 v[82:85], v86 offset:64\n"
    "ds_read_b128 v[86:89], v86 offset:128\n"
    "ds_read_b128 v[90:93], v98\n"
    "ds_read_b128 v[94:97], v98 offset:16\n"
    "ds_read_b128 v[98:101], v98 offset:32\n"
    "s_waitcnt lgkmcnt(5)\n"
    "v_mfma_f32_16x16x32_bf16 v[30:33], v[78:81], v[30:33], 0\n"
    "s_waitcnt lgkmcnt(4)\n"
    "v_mfma_f32_16x16x32_bf16 v[30:33], v[82:85], v[34:37], v[30:33]\n"
    "s_waitcnt lgkmcnt(3)\n"
    "v_mfma_f32_16x16x32_bf16 v[30:33], v[86:89], v[62:65], v[30:33]\n"
    "s_waitcnt lgkmcnt(2)\n"
    "v_add_f32_e32 v0, v90, v92\n"
    "s_waitcnt lgkmcnt(1)\n"
    "v_add_f32_e32 v1, v94, v96\n"
    "v_mfma_f32_16x16x32_bf16 v[34:37], v[78:81], v[38:41], 0\n"
    "v_add_f32_e32 v0, v0, v1\n"
    "s_waitcnt lgkmcnt(0)\n"
    "v_add_f32_e32 v26, v98, v100\n"
    "v_mfma_f32_16x16x32_bf16 v[34:37], v[82:85], v[42:45], v[34:37]\n"
    "v_add_f32_e32 v1, v91, v93\n"
    "v_add_f32_e32 v27, v95, v97\n"
    "v_mfma_f32_16x16x32_bf16 v[34:37], v[86:89], v[66:69], v[34:37]\n"
    "v_add_f32_e32 v0, v0, v26\n"
    "v_add_f32_e32 v28, v99, v101\n"
    "v_mfma_f32_16x16x32_bf16 v[38:41], v[78:81], v[46:49], 0\n"
    "v_add_f32_e32 v1, v1, v27\n"
    "v_fma_f32 v26, -v0, v0, v28\n"
    "v_mfma_f32_16x16x32_bf16 v[38:41], v[82:85], v[50:53], v[38:41]\n"
    "v_add_f32_e32 v1, v1, v26\n"
    "v_maximum3_f32 v1, v1, 0, 0\n"
    "v_mfma_f32_16x16x32_bf16 v[38:41], v[86:89], v[70:73], v[38:41]\n"
    "v_add_f32_e32 v1, 0x3727c5ac, v1\n"
    "v_rsq_f32_e32 v1, v1\n"
    "v_mfma_f32_16x16x32_bf16 v[42:45], v[78:81], v[54:57], 0\n"
    "v_fma_f32 v26, -v0, v31, v30\n"
    "v_fmac_f32_e32 v32, v1, v26\n"
    "v_mfma_f32_16x16x32_bf16 v[42:45], v[82:85], v[58:61], v[42:45]\n"
    "v_fma_f32 v26, -v0, v35, v34\n"
    "v_fmac_f32_e32 v36, v1, v26\n"
    "v_mfma_f32_16x16x32_bf16 v[42:45], v[86:89], v[74:77], v[42:45]\n"
    "s_mov_b32 vcc_lo, 0xffff0000\n"
    "s_mov_b32 vcc_hi, vcc_lo\n"
    "v_cndmask_b32_e32 v26, v32, v36, vcc\n"
    "v_fma_f32 v27, -v0, v39, v38\n"
    "v_fmac_f32_e32 v40, v1, v27\n"
    "s_nop 2\n"
    "v_fma_f32 v0, -v0, v43, v42\n"
    "v_fmac_f32_e32 v44, v1, v0\n"
    "v_cndmask_b32_e32 v0, v40, v44, vcc\n"
    "v_cndmask_b32_e64 v0, v26, v0, s[0:1]\n"
    "ds_write_b32 v102, v0\n"
    "s_waitcnt lgkmcnt(0)\n"
    "s_barrier\n"
    "s_waitcnt lgkmcnt(0)\n"
    "s_barrier\n"
    "ds_read_b128 v[30:33], v103\n"
    "ds_read_b128 v[34:37], v103 offset:64\n"
    "s_xor_b64 s[2:3], s[0:1], -1\n"
    "s_waitcnt lgkmcnt(1)\n"
    "v_mfma_f32_16x16x32_bf16 v[0:3], v[30:33], v[2:5], 0\n"
    "s_waitcnt lgkmcnt(0)\n"
    "v_mfma_f32_16x16x32_bf16 v[0:3], v[34:37], v[6:9], v[0:3]\n"
    "v_mfma_f32_16x16x32_bf16 v[2:5], v[30:33], v[10:13], 0\n"
    "v_mfma_f32_16x16x32_bf16 v[2:5], v[34:37], v[14:17], v[2:5]\n"
    "v_mfma_f32_16x16x32_bf16 v[4:7], v[30:33], v[22:25], 0\n"
    "v_mfma_f32_16x16x32_bf16 v[4:7], v[34:37], v[18:21], v[4:7]\n"
    "s_and_saveexec_b64 s[0:1], s[2:3]\n"
    "s_nop 6\n"
    "v_cndmask_b32_e32 v4, v0, v2, vcc\n"
    "s_or_b64 exec, exec, s[0:1]\n"
    "buffer_store_dword v4, v29, s[4:7], 0 offen sc1\n"
    "s_endpgm\n"
    ".p2align 7\n"
    ".Lfd_R_AM:\n"
    "s_barrier\n"
    "s_waitcnt lgkmcnt(0)\n"
    "s_mov_b32 s4, s94\n"
    "s_memrealtime s[8:9]\n"
    "s_load_dwordx2 s[6:7], s[0:1], 0x0\n"
    "s_waitcnt lgkmcnt(0)\n"
    "s_sub_i32 s3, s8, s4\n"
    "s_cmpk_gt_u32 s3, 0xb3\n"
    "v_readfirstlane_b32 s3, v0\n"
    "s_cbranch_scc1 .Lfd_R_AM_2\n"
    ".Lfd_R_AM_1:\n"
    "s_sleep 2\n"
    "s_memrealtime s[8:9]\n"
    "s_waitcnt lgkmcnt(0)\n"
    "s_sub_i32 s5, s8, s4\n"
    "s_cmpk_lt_u32 s5, 0xb4\n"
    "s_cbranch_scc1 .Lfd_R_AM_1\n"
    ".Lfd_R_AM_2:\n"
    "s_load_dwordx2 s[4:5], s[0:1], 0x30\n"
    "s_and_b32 s1, s2, 7\n"
    "v_and_b32_e32 v62, 63, v0\n"
    "s_lshr_b32 s0, s3, 6\n"
    "s_lshl_b32 s1, s1, 2\n"
    "s_bfe_u32 s8, s2, 0x20005\n"
    "s_or_b32 s1, s1, s8\n"
    "s_add_i32 s13, s2, 0xffffff80\n"
    "v_lshlrev_b32_e32 v63, 4, v62\n"
    "s_lshl_b32 s8, s0, 12\n"
    "v_lshl_or_b32 v4, s13, 14, v63\n"
    "v_mov_b32_e32 v6, s8\n"
    "s_movk_i32 s9, 0xc0\n"
    "v_or_b32_e32 v5, s8, v4\n"
    "s_movk_i32 s14, 0x80\n"
    "v_bitop3_b32 v9, v4, s9, v6 bitop3:0x36\n"
    "s_mov_b32 s9, 0x200010\n"
    "s_lshl_b32 s10, s0, 15\n"
    "v_bitop3_b32 v7, v4, 64, s8 bitop3:0x36\n"
    "v_bitop3_b32 v8, v4, s14, v6 bitop3:0x36\n"
    "s_xor_b32 m0, s10, 0\n"
    "s_waitcnt vmcnt(63)\n"
    "global_load_lds_dwordx4 v5, s[6:7] nt\n"
    "global_load_lds_dwordx4 v7, s[6:7] offset:1024 nt\n"
    "global_load_lds_dwordx4 v8, s[6:7] offset:2048 nt\n"
    "global_load_lds_dwordx4 v9, s[6:7] offset:3072 nt\n"
    "v_bitop3_b32 v5, v4, s9, v6 bitop3:0x36\n"
    "s_mov_b32 s9, 0x200050\n"
    "v_bitop3_b32 v7, v4, s9, v6 bitop3:0x36\n"
    "s_mov_b32 s9, 0x200090\n"
    "v_bitop3_b32 v8, v4, s9, v6 bitop3:0x36\n"
    "s_mov_b32 s9, 0x2000d0\n"
    "v_bitop3_b32 v9, v4, s9, v6 bitop3:0x36\n"
    "s_mov_b32 s9, 0x400020\n"
    "s_xor_b32 m0, s10, 0x1000\n"
    "s_waitcnt vmcnt(6)\n"
    "global_load_lds_dwordx4 v5, s[6:7] nt\n"
    "global_load_lds_dwordx4 v7, s[6:7] offset:1024 nt\n"
    "global_load_lds_dwordx4 v8, s[6:7] offset:2048 nt\n"
    "global_load_lds_dwordx4 v9, s[6:7] offset:3072 nt\n"
    "v_bitop3_b32 v5, v4, s9, v6 bitop3:0x36\n"
    "s_mov_b32 s9, 0x400060\n"
    "v_bitop3_b32 v7, v4, s9, v6 bitop3:0x36\n"
    "s_mov_b32 s9, 0x4000a0\n"
    "v_bitop3_b32 v8, v4, s9, v6 bitop3:0x36\n"
    "s_mov_b32 s9, 0x4000e0\n"
    "v_bitop3_b32 v9, v4, s9, v6 bitop3:0x36\n"
    "s_mov_b32 s9, 0x600030\n"
    "s_xor_b32 m0, s10, 0x2000\n"
    "s_waitcnt vmcnt(6)\n"
    "global_load_lds_dwordx4 v5, s[6:7] nt\n"
    "global_load_lds_dwordx4 v7, s[6:7] offset:1024 nt\n"
    "global_load_lds_dwordx4 v8, s[6:7] offset:2048 nt\n"
    "global_load_lds_dwordx4 v9, s[6:7] offset:3072 nt\n"
    "v_bitop3_b32 v5, v4, s9, v6 bitop3:0x36\n"
    "s_mov_b32 s9, 0x600070\n"
    "v_bitop3_b32 v7, v4, s9, v6 bitop3:0x36\n"
    "s_mov_b32 s9, 0x6000b0\n"
    "v_bitop3_b32 v8, v4, s9, v6 bitop3:0x36\n"
    "s_mov_b32 s9, 0x6000f0\n"
    "v_bitop3_b32 v9, v4, s9, v6 bitop3:0x36\n"
    "s_mov_b32 s9, 0x800000\n"
    "s_xor_b32 m0, s10, 0x3000\n"
    "s_waitcnt vmcnt(6)\n"
    "global_load_lds_dwordx4 v5, s[6:7] nt\n"
    "global_load_lds_dwordx4 v7, s[6:7] offset:1024 nt\n"
    "global_load_lds_dwordx4 v8, s[6:7] offset:2048 nt\n"
    "global_load_lds_dwordx4 v9, s[6:7] offset:3072 nt\n"
    "v_bitop3_b32 v5, v4, s9, v6 bitop3:0x36\n"
    "s_mov_b32 s9, 0x800040\n"
    "v_bitop3_b32 v7, v4, s9, v6 bitop3:0x36\n"
    "s_mov_b32 s9, 0x800080\n"
    "v_bitop3_b32 v8, v4, s9, v6 bitop3:0x36\n"
    "s_mov_b32 s9, 0x8000c0\n"
    "v_bitop3_b32 v9, v4, s9, v6 bitop3:0x36\n"
    "s_mov_b32 s9, 0xa00010\n"
    "s_xor_b32 m0, s10, 0x4000\n"
    "s_waitcnt vmcnt(6)\n"
    "global_load_lds_dwordx4 v5, s[6:7] nt\n"
    "global_load_lds_dwordx4 v7, s[6:7] offset:1024 nt\n"
    "global_load_lds_dwordx4 v8, s[6:7] offset:2048 nt\n"
    "global_load_lds_dwordx4 v9, s[6:7] offset:3072 nt\n"
    "v_bitop3_b32 v5, v4, s9, v6 bitop3:0x36\n"
    "s_mov_b32 s9, 0xa00050\n"
    "v_bitop3_b32 v7, v4, s9, v6 bitop3:0x36\n"
    "s_mov_b32 s9, 0xa00090\n"
    "v_bitop3_b32 v8, v4, s9, v6 bitop3:0x36\n"
    "s_mov_b32 s9, 0xa000d0\n"
    "v_bitop3_b32 v9, v4, s9, v6 bitop3:0x36\n"
    "s_mov_b32 s9, 0xc00020\n"
    "s_xor_b32 m0, s10, 0x5000\n"
    "s_waitcnt vmcnt(6)\n"
    "global_load_lds_dwordx4 v5, s[6:7] nt\n"
    "global_load_lds_dwordx4 v7, s[6:7] offset:1024 nt\n"
    "global_load_lds_dwordx4 v8, s[6:7] offset:2048 nt\n"
    "global_load_lds_dwordx4 v9, s[6:7] offset:3072 nt\n"
    "v_bitop3_b32 v5, v4, s9, v6 bitop3:0x36\n"
    "s_mov_b32 s9, 0xc00060\n"
    "v_bitop3_b32 v7, v4, s9, v6 bitop3:0x36\n"
    "s_mov_b32 s9, 0xc000a0\n"
    "v_bitop3_b32 v8, v4, s9, v6 bitop3:0x36\n"
    "s_mov_b32 s9, 0xc000e0\n"
    "v_bitop3_b32 v9, v4, s9, v6 bitop3:0x36\n"
    "s_mov_b32 s9, 0xe00030\n"
    "s_xor_b32 m0, s10, 0x6000\n"
    "s_waitcnt vmcnt(6)\n"
    "global_load_lds_dwordx4 v5, s[6:7] nt\n"
    "global_load_lds_dwordx4 v7, s[6:7] offset:1024 nt\n"
    "global_load_lds_dwordx4 v8, s[6:7] offset:2048 nt\n"
    "global_load_lds_dwordx4 v9, s[6:7] offset:3072 nt\n"
    "v_bitop3_b32 v5, v4, s9, v6 bitop3:0x36\n"
    "s_mov_b32 s9, 0xe00070\n"
    "v_bitop3_b32 v7, v4, s9, v6 bitop3:0x36\n"
    "s_mov_b32 s9, 0xe000b0\n"
    "v_bitop3_b32 v8, v4, s9, v6 bitop3:0x36\n"
    "s_mov_b32 s9, 0xe000f0\n"
    "v_bitop3_b32 v4, v4, s9, v6 bitop3:0x36\n"
    "s_xor_b32 m0, s10, 0x7000\n"
    "s_waitcnt vmcnt(6)\n"
    "global_load_lds_dwordx4 v5, s[6:7] nt\n"
    "global_load_lds_dwordx4 v7, s[6:7] offset:1024 nt\n"
    "global_load_lds_dwordx4 v8, s[6:7] offset:2048 nt\n"
    "global_load_lds_dwordx4 v4, s[6:7] offset:3072 nt\n"
    "s_barrier\n"
    "v_mov_b32_e32 v4, 0x27fd4\n"
    "v_mov_b32_e32 v5, 0x27ff0\n"
    "ds_read_b32 v4, v4\n"
    "ds_read_b32 v5, v5\n"
    "v_and_b32_e32 v3, 31, v0\n"
    "v_bfe_u32 v1, v0, 5, 1\n"
    "v_bfe_u32 v6, v0, 2, 2\n"
    "s_waitcnt lgkmcnt(0)\n"
    "v_readfirstlane_b32 s9, v4\n"
    "v_readfirstlane_b32 s11, v5\n"
    "v_lshlrev_b32_e32 v5, 2, v0\n"
    "v_and_b32_e32 v5, 12, v5\n"
    "v_lshlrev_b32_e32 v4, 10, v3\n"
    "v_bitop3_b32 v5, v5, v1, v6 bitop3:0x36\n"
    "s_mov_b32 s7, 0x20000\n"
    "v_lshl_or_b32 v5, v5, 4, v4\n"
    "v_mov_b32_e32 v6, 0x60\n"
    "v_bitop3_b32 v138, v5, s10, v6 bitop3:0xde\n"
    "v_bitop3_b32 v146, v5, s7, v6 bitop3:0xde\n"
    "v_mov_b32_e32 v6, 0x80\n"
    "v_bitop3_b32 v140, v5, s10, v6 bitop3:0xde\n"
    "v_bitop3_b32 v147, v5, s7, v6 bitop3:0xde\n"
    "v_mov_b32_e32 v6, 0xa0\n"
    "v_bitop3_b32 v148, v5, s10, v6 bitop3:0xde\n"
    "v_bitop3_b32 v149, v5, s7, v6 bitop3:0xde\n"
    "v_mov_b32_e32 v6, 0xc0\n"
    "v_bitop3_b32 v150, v5, s10, v6 bitop3:0xde\n"
    "v_bitop3_b32 v151, v5, s7, v6 bitop3:0xde\n"
    "v_mov_b32_e32 v6, 0xe0\n"
    "v_and_b32_e32 v2, 15, v0\n"
    "v_or_b32_e32 v29, s10, v5\n"
    "v_or_b32_e32 v71, 0x20000, v5\n"
    "v_bitop3_b32 v136, v5, s10, 32 bitop3:0xde\n"
    "v_bitop3_b32 v144, v5, s7, 32 bitop3:0xde\n"
    "v_bitop3_b32 v137, v5, s10, 64 bitop3:0xde\n"
    "v_bitop3_b32 v145, v5, s7, 64 bitop3:0xde\n"
    "v_bitop3_b32 v152, v5, s10, v6 bitop3:0xde\n"
    "v_bitop3_b32 v153, v5, s7, v6 bitop3:0xde\n"
    "v_lshlrev_b32_e32 v5, 6, v0\n"
    "v_and_b32_e32 v6, 0x400, v5\n"
    "v_lshlrev_b32_e32 v7, 4, v2\n"
    "s_mov_b32 s10, 0x27800\n"
    "v_bfe_u32 v64, v0, 3, 3\n"
    "v_or3_b32 v26, v6, v7, s10\n"
    "v_and_b32_e32 v7, 2, v64\n"
    "v_lshrrev_b32_e32 v8, 1, v0\n"
    "s_lshl_b32 s10, s0, 2\n"
    "s_lshl_b32 s15, s0, 11\n"
    "v_lshlrev_b32_e32 v6, 8, v0\n"
    "v_and_or_b32 v7, v8, 1, v7\n"
    "v_and_or_b32 v8, v0, 12, v1\n"
    "s_and_b32 s5, s5, 0xffff\n"
    "s_lshl_b32 s12, s0, 1\n"
    "s_add_i32 s16, s15, 0x20000\n"
    "v_lshl_add_u32 v3, v3, 4, s10\n"
    "v_and_b32_e32 v6, 0xc00, v6\n"
    "s_lshl_b32 s18, s0, 8\n"
    "v_and_b32_e32 v65, 1, v0\n"
    "v_bitop3_b32 v7, v7, v8, 8 bitop3:0x36\n"
    "v_add_u32_e32 v24, 0x27000, v3\n"
    "v_lshlrev_b32_e32 v3, 12, v1\n"
    "v_lshlrev_b32_e32 v7, 4, v7\n"
    "v_lshl_or_b32 v6, v65, 3, v6\n"
    "s_bitcmp0_b32 s11, 0\n"
    "v_or_b32_e32 v25, s16, v63\n"
    "v_or3_b32 v3, v6, v3, v7\n"
    "s_cselect_b64 s[16:17], -1, 0\n"
    "v_add_u32_e32 v154, s18, v3\n"
    "v_cndmask_b32_e64 v27, 2.0, 1.0, s[16:17]\n"
    "v_lshlrev_b32_e32 v66, 9, v0\n"
    "s_mov_b32 s16, 0x76000\n"
    "v_mov_b32_e32 v3, 0x74000\n"
    "v_bitop3_b32 v3, v66, s16, v3 bitop3:0xc8\n"
    "s_lshl_b32 s16, s13, 3\n"
    "s_and_b32 s16, s16, 0xffffff80\n"
    "s_add_i32 s15, s16, s15\n"
    "s_addk_i32 s15, 0x400\n"
    "v_mov_b32_e32 v67, 0x7ff00000\n"
    "v_cmp_lt_u32_e32 vcc, 31, v62\n"
    "v_add_u32_e32 v3, s15, v3\n"
    "v_lshlrev_b32_e32 v2, 2, v2\n"
    "v_cndmask_b32_e32 v6, 0, v67, vcc\n"
    "v_or3_b32 v23, v3, v2, v6\n"
    "v_and_b32_e32 v3, 7, v0\n"
    "v_add_u32_e32 v2, s18, v4\n"
    "v_lshlrev_b32_e32 v3, 4, v3\n"
    "v_and_or_b32 v2, v5, s14, v2\n"
    "v_bitop3_b32 v4, v3, v0, 32 bitop3:0x78\n"
    "v_bitop3_b32 v69, v2, s14, v4 bitop3:0x36\n"
    "v_lshl_add_u32 v2, v64, 10, s18\n"
    "v_lshlrev_b32_e32 v4, 3, v0\n"
    "v_and_or_b32 v2, v4, s14, v2\n"
    "s_lshl_b32 s13, s13, 8\n"
    "v_bitop3_b32 v70, v2, s14, v3 bitop3:0x36\n"
    "s_and_b32 s13, s13, 0xffffc000\n"
    "v_bitop3_b32 v3, v64, s2, 63 bitop3:0x78\n"
    "v_bitop3_b32 v4, v64, v0, 7 bitop3:0x78\n"
    "v_and_b32_e32 v2, 56, v0\n"
    "s_add_i32 s13, s13, 0x208000\n"
    "v_lshlrev_b32_e32 v3, 7, v3\n"
    "v_lshlrev_b32_e32 v4, 4, v4\n"
    "v_add_u32_e32 v2, s12, v2\n"
    "v_or3_b32 v3, s13, v3, v4\n"
    "s_barrier\n"
    "v_or_b32_e32 v22, 0x20000, v63\n"
    "v_add_u32_e32 v28, s10, v26\n"
    "v_lshl_add_u32 v68, v2, 15, v3\n"
    "s_mov_b32 s6, 0x600000\n"
    "s_waitcnt vmcnt(0)\n"
    "ds_read_b128 v[2:5], v29\n"
    "ds_read_b128 v[18:21], v29 offset:256\n"
    "ds_read_b128 v[30:33], v136\n"
    "ds_read_b128 v[34:37], v136 offset:256\n"
    "ds_read_b128 v[38:41], v137\n"
    "ds_read_b128 v[42:45], v137 offset:256\n"
    "ds_read_b128 v[46:49], v138\n"
    "ds_read_b128 v[50:53], v138 offset:256\n"
    "ds_read_b128 v[54:57], v140\n"
    "ds_read_b128 v[58:61], v140 offset:256\n"
    "ds_read_b128 v[72:75], v148\n"
    "ds_read_b128 v[76:79], v148 offset:256\n"
    "ds_read_b128 v[80:83], v150\n"
    "ds_read_b128 v[84:87], v150 offset:256\n"
    "ds_read_b128 v[88:91], v152\n"
    "ds_read_b128 v[92:95], v152 offset:256\n"
    "ds_read_b128 v[96:99], v29 offset:512\n"
    "ds_read_b128 v[100:103], v136 offset:512\n"
    "s_movk_i32 s13, 0xc00\n"
    "s_setprio 2\n"
    "s_waitcnt lgkmcnt(0)\n"
    "s_barrier\n"
    "ds_read_b128 v[6:9], v71\n"
    "ds_read_b128 v[104:107], v144\n"
    "ds_read_b128 v[108:111], v145\n"
    "ds_read_b128 v[112:115], v146\n"
    "ds_read_b128 v[116:119], v147\n"
    "ds_read_b128 v[120:123], v149\n"
    "ds_read_b128 v[124:127], v137 offset:512\n"
    "s_waitcnt lgkmcnt(6)\n"
    "v_mfma_f32_32x32x16_bf16 v[2:17], v[2:5], v[6:9], 0\n"
    "ds_read_b128 v[128:131], v151\n"
    "ds_read_b128 v[132:135], v138 offset:512\n"
    "s_waitcnt lgkmcnt(7)\n"
    "v_mfma_f32_32x32x16_bf16 v[2:17], v[30:33], v[104:107], v[2:17]\n"
    "ds_read_b128 v[30:33], v153\n"
    "ds_read_b128 v[104:107], v140 offset:512\n"
    "s_waitcnt lgkmcnt(8)\n"
    "v_mfma_f32_32x32x16_bf16 v[2:17], v[38:41], v[108:111], v[2:17]\n"
    "ds_read_b128 v[38:41], v71 offset:256\n"
    "ds_read_b128 v[108:111], v148 offset:512\n"
    "s_waitcnt lgkmcnt(9)\n"
    "v_mfma_f32_32x32x16_bf16 v[2:17], v[46:49], v[112:115], v[2:17]\n"
    "ds_read_b128 v[46:49], v144 offset:256\n"
    "ds_read_b128 v[112:115], v150 offset:512\n"
    "s_waitcnt lgkmcnt(10)\n"
    "v_mfma_f32_32x32x16_bf16 v[2:17], v[54:57], v[116:119], v[2:17]\n"
    "ds_read_b128 v[54:57], v145 offset:256\n"
    "ds_read_b128 v[116:119], v152 offset:512\n"
    "s_waitcnt lgkmcnt(11)\n"
    "v_mfma_f32_32x32x16_bf16 v[2:17], v[72:75], v[120:123], v[2:17]\n"
    "ds_read_b128 v[72:75], v146 offset:256\n"
    "ds_read_b128 v[120:123], v29 offset:768\n"
    "s_waitcnt lgkmcnt(11)\n"
    "v_mfma_f32_32x32x16_bf16 v[2:17], v[80:83], v[128:131], v[2:17]\n"
    "ds_read_b128 v[80:83], v147 offset:256\n"
    "ds_read_b128 v[128:131], v136 offset:768\n"
    "s_waitcnt lgkmcnt(11)\n"
    "v_mfma_f32_32x32x16_bf16 v[2:17], v[88:91], v[30:33], v[2:17]\n"
    "ds_read_b128 v[30:33], v149 offset:256\n"
    "ds_read_b128 v[88:91], v137 offset:768\n"
    "s_waitcnt lgkmcnt(11)\n"
    "v_mfma_f32_32x32x16_bf16 v[2:17], v[18:21], v[38:41], v[2:17]\n"
    "ds_read_b128 v[18:21], v151 offset:256\n"
    "ds_read_b128 v[136:139], v138 offset:768\n"
    "s_waitcnt lgkmcnt(11)\n"
    "v_mfma_f32_32x32x16_bf16 v[2:17], v[34:37], v[46:49], v[2:17]\n"
    "ds_read_b128 v[34:37], v153 offset:256\n"
    "ds_read_b128 v[140:143], v140 offset:768\n"
    "s_waitcnt lgkmcnt(11)\n"
    "v_mfma_f32_32x32x16_bf16 v[2:17], v[42:45], v[54:57], v[2:17]\n"
    "ds_read_b128 v[38:41], v71 offset:512\n"
    "ds_read_b128 v[54:57], v148 offset:768\n"
    "s_waitcnt lgkmcnt(11)\n"
    "v_mfma_f32_32x32x16_bf16 v[2:17], v[50:53], v[72:75], v[2:17]\n"
    "ds_read_b128 v[42:45], v144 offset:512\n"
    "ds_read_b128 v[72:75], v150 offset:768\n"
    "s_waitcnt lgkmcnt(11)\n"
    "v_mfma_f32_32x32x16_bf16 v[2:17], v[58:61], v[80:83], v[2:17]\n"
    "ds_read_b128 v[46:49], v145 offset:512\n"
    "ds_read_b128 v[80:83], v152 offset:768\n"
    "s_waitcnt lgkmcnt(11)\n"
    "v_mfma_f32_32x32x16_bf16 v[2:17], v[76:79], v[30:33], v[2:17]\n"
    "ds_read_b128 v[30:33], v146 offset:512\n"
    "s_waitcnt lgkmcnt(10)\n"
    "v_mfma_f32_32x32x16_bf16 v[2:17], v[84:87], v[18:21], v[2:17]\n"
    "ds_read_b128 v[18:21], v147 offset:512\n"
    "s_waitcnt lgkmcnt(9)\n"
    "v_mfma_f32_32x32x16_bf16 v[2:17], v[92:95], v[34:37], v[2:17]\n"
    "ds_read_b128 v[34:37], v149 offset:512\n"
    "v_mov_b32_e32 v29, v154\n"
    "s_waitcnt lgkmcnt(8)\n"
    "v_mfma_f32_32x32x16_bf16 v[2:17], v[96:99], v[38:41], v[2:17]\n"
    "ds_read_b128 v[38:41], v151 offset:512\n"
    "v_xor_b32_e32 v50, 32, v154\n"
    "v_mov_b32_e32 v96, v50\n"
    "s_waitcnt lgkmcnt(7)\n"
    "v_mfma_f32_32x32x16_bf16 v[2:17], v[100:103], v[42:45], v[2:17]\n"
    "ds_read_b128 v[42:45], v153 offset:512\n"
    "v_add_u32_e32 v97, 0x10000, v154\n"
    "s_waitcnt lgkmcnt(6)\n"
    "v_mfma_f32_32x32x16_bf16 v[2:17], v[124:127], v[46:49], v[2:17]\n"
    "ds_read_b128 v[46:49], v71 offset:768\n"
    "v_add_u32_e32 v71, 0x10000, v50\n"
    "s_waitcnt lgkmcnt(5)\n"
    "v_mfma_f32_32x32x16_bf16 v[2:17], v[132:135], v[30:33], v[2:17]\n"
    "ds_read_b128 v[30:33], v144 offset:768\n"
    "v_xor_b32_e32 v76, 64, v154\n"
    "v_mov_b32_e32 v98, v76\n"
    "s_waitcnt lgkmcnt(5)\n"
    "v_mfma_f32_32x32x16_bf16 v[2:17], v[104:107], v[18:21], v[2:17]\n"
    "ds_read_b128 v[50:53], v145 offset:768\n"
    "v_xor_b32_e32 v18, 0x60, v154\n"
    "v_mov_b32_e32 v99, v18\n"
    "s_waitcnt lgkmcnt(5)\n"
    "v_mfma_f32_32x32x16_bf16 v[2:17], v[108:111], v[34:37], v[2:17]\n"
    "s_barrier\n"
    "ds_read_b128 v[58:61], v146 offset:768\n"
    "v_add_u32_e32 v100, 0x10000, v76\n"
    "s_waitcnt lgkmcnt(5)\n"
    "v_mfma_f32_32x32x16_bf16 v[2:17], v[112:115], v[38:41], v[2:17]\n"
    "ds_read_b128 v[76:79], v147 offset:768\n"
    "v_add_u32_e32 v101, 0x10000, v18\n"
    "s_waitcnt lgkmcnt(5)\n"
    "v_mfma_f32_32x32x16_bf16 v[2:17], v[116:119], v[42:45], v[2:17]\n"
    "ds_read_b128 v[84:87], v149 offset:768\n"
    "ds_read_b64_tr_b16 v[18:19], v29\n"
    "ds_read_b64_tr_b16 v[20:21], v96 offset:8192\n"
    "s_waitcnt lgkmcnt(7)\n"
    "v_mfma_f32_32x32x16_bf16 v[2:17], v[120:123], v[46:49], v[2:17]\n"
    "ds_read_b128 v[92:95], v151 offset:768\n"
    "ds_read_b64_tr_b16 v[34:35], v29 offset:16384\n"
    "ds_read_b64_tr_b16 v[36:37], v96 offset:24576\n"
    "s_waitcnt lgkmcnt(9)\n"
    "v_mfma_f32_32x32x16_bf16 v[2:17], v[128:131], v[30:33], v[2:17]\n"
    "ds_read_b128 v[30:33], v153 offset:768\n"
    "ds_read_b64_tr_b16 v[38:39], v29 offset:32768\n"
    "ds_read_b64_tr_b16 v[40:41], v96 offset:40960\n"
    "s_waitcnt lgkmcnt(11)\n"
    "v_mfma_f32_32x32x16_bf16 v[2:17], v[88:91], v[50:53], v[2:17]\n"
    "ds_read_b64_tr_b16 v[42:43], v29 offset:49152\n"
    "ds_read_b64_tr_b16 v[44:45], v96 offset:57344\n"
    "s_waitcnt lgkmcnt(12)\n"
    "v_mfma_f32_32x32x16_bf16 v[2:17], v[136:139], v[58:61], v[2:17]\n"
    "ds_read_b64_tr_b16 v[46:47], v97\n"
    "ds_read_b64_tr_b16 v[48:49], v71 offset:8192\n"
    "s_waitcnt lgkmcnt(13)\n"
    "v_mfma_f32_32x32x16_bf16 v[2:17], v[140:143], v[76:79], v[2:17]\n"
    "ds_read_b64_tr_b16 v[50:51], v97 offset:16384\n"
    "ds_read_b64_tr_b16 v[52:53], v71 offset:24576\n"
    "s_waitcnt lgkmcnt(14)\n"
    "v_mfma_f32_32x32x16_bf16 v[2:17], v[54:57], v[84:87], v[2:17]\n"
    "ds_read_b64_tr_b16 v[54:55], v97 offset:32768\n"
    "ds_read_b64_tr_b16 v[56:57], v71 offset:40960\n"
    "s_waitcnt lgkmcnt(13)\n"
    "v_mfma_f32_32x32x16_bf16 v[2:17], v[72:75], v[92:95], v[2:17]\n"
    "ds_read_b64_tr_b16 v[58:59], v97 offset:49152\n"
    "ds_read_b64_tr_b16 v[60:61], v71 offset:57344\n"
    "s_waitcnt lgkmcnt(12)\n"
    "v_mfma_f32_32x32x16_bf16 v[2:17], v[80:83], v[30:33], v[2:17]\n"
    "s_nop 11\n"
    "v_maximum3_f32 v29, v2, v3, v3\n"
    "v_maximum3_f32 v30, v5, v6, v7\n"
    "v_maximum3_f32 v31, v11, v12, v13\n"
    "v_maximum3_f32 v32, v14, v15, v16\n"
    "v_maximum3_f32 v29, v29, v4, v30\n"
    "v_maximum3_f32 v30, v8, v9, v10\n"
    "v_maximum3_f32 v31, v31, v32, v17\n"
    "v_maximum3_f32 v29, v29, v30, v31\n"
    "v_mov_b32_e32 v30, v29\n"
    "s_nop 1\n"
    "v_permlane32_swap_b32_e32 v29, v30\n"
    "v_maximum3_f32 v30, v29, v30, v30\n"
    "ds_write_b32 v28, v30\n"
    "v_pk_add_f32 v[2:3], v[2:3], v[30:31] op_sel_hi:[1,0] neg_lo:[0,1] neg_hi:[0,1]\n"
    "v_pk_add_f32 v[4:5], v[4:5], v[30:31] op_sel_hi:[1,0] neg_lo:[0,1] neg_hi:[0,1]\n"
    "v_pk_add_f32 v[6:7], v[6:7], v[30:31] op_sel_hi:[1,0] neg_lo:[0,1] neg_hi:[0,1]\n"
    "v_pk_add_f32 v[8:9], v[8:9], v[30:31] op_sel_hi:[1,0] neg_lo:[0,1] neg_hi:[0,1]\n"
    "v_pk_add_f32 v[10:11], v[10:11], v[30:31] op_sel_hi:[1,0] neg_lo:[0,1] neg_hi:[0,1]\n"
    "v_exp_f32_e32 v28, v2\n"
    "v_exp_f32_e32 v29, v3\n"
    "v_exp_f32_e32 v32, v4\n"
    "v_exp_f32_e32 v33, v5\n"
    "v_pk_add_f32 v[12:13], v[12:13], v[30:31] op_sel_hi:[1,0] neg_lo:[0,1] neg_hi:[0,1]\n"
    "v_pk_add_f32 v[14:15], v[14:15], v[30:31] op_sel_hi:[1,0] neg_lo:[0,1] neg_hi:[0,1]\n"
    "v_pk_add_f32 v[16:17], v[16:17], v[30:31] op_sel_hi:[1,0] neg_lo:[0,1] neg_hi:[0,1]\n"
    "s_waitcnt lgkmcnt(0)\n"
    "s_barrier\n"
    "ds_read_b128 v[2:5], v26\n"
    "v_exp_f32_e32 v6, v6\n"
    "v_exp_f32_e32 v7, v7\n"
    "v_exp_f32_e32 v8, v8\n"
    "v_exp_f32_e32 v9, v9\n"
    "v_exp_f32_e32 v10, v10\n"
    "v_exp_f32_e32 v11, v11\n"
    "v_exp_f32_e32 v12, v12\n"
    "v_exp_f32_e32 v13, v13\n"
    "v_exp_f32_e32 v14, v14\n"
    "v_exp_f32_e32 v15, v15\n"
    "v_exp_f32_e32 v16, v16\n"
    "v_exp_f32_e32 v17, v17\n"
    "s_waitcnt lgkmcnt(0)\n"
    "v_maximum3_f32 v2, v2, v3, v3\n"
    "v_maximum3_f32 v2, v2, v4, v5\n"
    "v_sub_f32_e32 v2, v30, v2\n"
    "v_exp_f32_e32 v2, v2\n"
    "s_nop 0\n"
    "v_mul_f32_e32 v26, v27, v2\n"
    "v_pk_mul_f32 v[2:3], v[26:27], v[28:29] op_sel_hi:[0,1]\n"
    "v_pk_mul_f32 v[4:5], v[26:27], v[32:33] op_sel_hi:[0,1]\n"
    "v_pk_mul_f32 v[6:7], v[26:27], v[6:7] op_sel_hi:[0,1]\n"
    "v_cvt_pk_bf16_f32 v2, v2, v3\n"
    "v_cvt_pk_bf16_f32 v3, v4, v5\n"
    "v_cvt_pk_bf16_f32 v4, v6, v7\n"
    "v_pk_mul_f32 v[6:7], v[26:27], v[8:9] op_sel_hi:[0,1]\n"
    "v_cvt_pk_bf16_f32 v5, v6, v7\n"
    "v_pk_mul_f32 v[6:7], v[26:27], v[10:11] op_sel_hi:[0,1]\n"
    "v_pk_mul_f32 v[8:9], v[26:27], v[12:13] op_sel_hi:[0,1]\n"
    "v_cvt_pk_bf16_f32 v6, v6, v7\n"
    "v_cvt_pk_bf16_f32 v7, v8, v9\n"
    "v_pk_mul_f32 v[8:9], v[26:27], v[14:15] op_sel_hi:[0,1]\n"
    "v_pk_mul_f32 v[10:11], v[26:27], v[16:17] op_sel_hi:[0,1]\n"
    "v_cvt_pk_bf16_f32 v8, v8, v9\n"
    "v_cvt_pk_bf16_f32 v9, v10, v11\n"
    "ds_write_b128 v25, v[2:5]\n"
    "ds_write_b128 v25, v[6:9] offset:1024\n"
    "s_waitcnt lgkmcnt(0)\n"
    "s_barrier\n"
    "s_nop 0\n"
    "v_lshlrev_b32_e32 v10, 16, v2\n"
    "v_and_b32_e32 v2, 0xffff0000, v2\n"
    "v_lshlrev_b32_e32 v11, 16, v3\n"
    "v_and_b32_e32 v3, 0xffff0000, v3\n"
    "v_add_f32_e32 v10, v10, v11\n"
    "v_add_f32_e32 v2, v2, v3\n"
    "v_lshlrev_b32_e32 v3, 16, v4\n"
    "v_and_b32_e32 v4, 0xffff0000, v4\n"
    "v_add_f32_e32 v3, v10, v3\n"
    "v_add_f32_e32 v2, v2, v4\n"
    "v_lshlrev_b32_e32 v4, 16, v5\n"
    "v_add_f32_e32 v3, v3, v4\n"
    "v_and_b32_e32 v4, 0xffff0000, v5\n"
    "v_add_f32_e32 v2, v2, v4\n"
    "v_lshlrev_b32_e32 v4, 16, v6\n"
    "v_add_f32_e32 v3, v3, v4\n"
    "v_and_b32_e32 v4, 0xffff0000, v6\n"
    "v_add_f32_e32 v2, v2, v4\n"
    "v_lshlrev_b32_e32 v4, 16, v7\n"
    "v_add_f32_e32 v3, v3, v4\n"
    "v_and_b32_e32 v4, 0xffff0000, v7\n"
    "v_add_f32_e32 v2, v2, v4\n"
    "v_lshlrev_b32_e32 v4, 16, v8\n"
    "v_add_f32_e32 v3, v3, v4\n"
    "v_and_b32_e32 v4, 0xffff0000, v8\n"
    "v_add_f32_e32 v2, v2, v4\n"
    "v_lshlrev_b32_e32 v4, 16, v9\n"
    "v_add_f32_e32 v3, v3, v4\n"
    "v_and_b32_e32 v4, 0xffff0000, v9\n"
    "v_add_f32_e32 v2, v2, v4\n"
    "v_add_f32_e32 v2, v2, v3\n"
    "v_mov_b32_e32 v3, v2\n"
    "s_nop 1\n"
    "v_permlane32_swap_b32_e32 v2, v3\n"
    "v_add_f32_e32 v2, v2, v3\n"
    "ds_write_b32 v24, v2\n"
    "s_sleep 1\n"
    "ds_read_b64_tr_b16 v[2:3], v98\n"
    "ds_read_b64_tr_b16 v[72:73], v98 offset:16384\n"
    "ds_read_b64_tr_b16 v[76:77], v98 offset:32768\n"
    "ds_read_b64_tr_b16 v[80:81], v98 offset:49152\n"
    "ds_read_b64_tr_b16 v[4:5], v99 offset:8192\n"
    "ds_read_b64_tr_b16 v[74:75], v99 offset:24576\n"
    "ds_read_b64_tr_b16 v[78:79], v99 offset:40960\n"
    "ds_read_b64_tr_b16 v[82:83], v99 offset:57344\n"
    "ds_read_b64_tr_b16 v[84:85], v100\n"
    "ds_read_b64_tr_b16 v[88:89], v100 offset:16384\n"
    "ds_read_b64_tr_b16 v[92:93], v100 offset:32768\n"
    "ds_read_b64_tr_b16 v[96:97], v100 offset:49152\n"
    "ds_read_b64_tr_b16 v[86:87], v101 offset:8192\n"
    "ds_read_b64_tr_b16 v[90:91], v101 offset:24576\n"
    "ds_read_b64_tr_b16 v[94:95], v101 offset:40960\n"
    "ds_read_b64_tr_b16 v[98:99], v101 offset:57344\n"
    "ds_read_b128 v[6:9], v22\n"
    "ds_read_b128 v[100:103], v22 offset:1024\n"
    "ds_read_b128 v[104:107], v22 offset:2048\n"
    "ds_read_b128 v[108:111], v22 offset:3072\n"
    "ds_read_b128 v[112:115], v22 offset:4096\n"
    "ds_read_b128 v[116:119], v22 offset:5120\n"
    "ds_read_b128 v[120:123], v22 offset:6144\n"
    "ds_read_b128 v[124:127], v22 offset:7168\n"
    "s_lshl_b32 s14, s9, 13\n"
    "v_mov_b32_e32 v10, 0x2000\n"
    "v_bitop3_b32 v71, v23, s14, v10 bitop3:0x78\n"
    "s_waitcnt lgkmcnt(7)\n"
    "v_mfma_f32_32x32x16_bf16 v[18:33], v[18:21], v[6:9], 0\n"
    "v_xor_b32_e32 v128, 16, v69\n"
    "s_waitcnt lgkmcnt(6)\n"
    "v_mfma_f32_32x32x16_bf16 v[18:33], v[34:37], v[100:103], v[18:33]\n"
    "s_waitcnt lgkmcnt(5)\n"
    "v_mfma_f32_32x32x16_bf16 v[18:33], v[38:41], v[104:107], v[18:33]\n"
    "s_waitcnt lgkmcnt(4)\n"
    "v_mfma_f32_32x32x16_bf16 v[18:33], v[42:45], v[108:111], v[18:33]\n"
    "s_waitcnt lgkmcnt(3)\n"
    "v_mfma_f32_32x32x16_bf16 v[18:33], v[46:49], v[112:115], v[18:33]\n"
    "s_waitcnt lgkmcnt(2)\n"
    "v_mfma_f32_32x32x16_bf16 v[18:33], v[50:53], v[116:119], v[18:33]\n"
    "s_waitcnt lgkmcnt(1)\n"
    "v_mfma_f32_32x32x16_bf16 v[18:33], v[54:57], v[120:123], v[18:33]\n"
    "s_waitcnt lgkmcnt(0)\n"
    "v_mfma_f32_32x32x16_bf16 v[18:33], v[58:61], v[124:127], v[18:33]\n"
    "v_mfma_f32_32x32x16_bf16 v[2:17], v[2:5], v[6:9], 0\n"
    "v_mfma_f32_32x32x16_bf16 v[2:17], v[72:75], v[100:103], v[2:17]\n"
    "v_mfma_f32_32x32x16_bf16 v[2:17], v[76:79], v[104:107], v[2:17]\n"
    "v_mfma_f32_32x32x16_bf16 v[2:17], v[80:83], v[108:111], v[2:17]\n"
    "s_nop 7\n"
    "v_cvt_pk_bf16_f32 v18, v18, v19\n"
    "v_cvt_pk_bf16_f32 v19, v20, v21\n"
    "v_cvt_pk_bf16_f32 v20, v22, v23\n"
    "v_cvt_pk_bf16_f32 v21, v24, v25\n"
    "ds_write_b128 v69, v[18:21]\n"
    "v_mfma_f32_32x32x16_bf16 v[2:17], v[84:87], v[112:115], v[2:17]\n"
    "v_cvt_pk_bf16_f32 v22, v26, v27\n"
    "v_cvt_pk_bf16_f32 v23, v28, v29\n"
    "v_cvt_pk_bf16_f32 v24, v30, v31\n"
    "v_cvt_pk_bf16_f32 v25, v32, v33\n"
    "ds_write_b128 v128, v[22:25]\n"
    "v_add_u32_e32 v18, v18, v19\n"
    "v_add3_u32 v18, v18, v20, v21\n"
    "v_add3_u32 v18, v18, v22, v23\n"
    "v_add3_u32 v26, v18, v24, v25\n"
    "v_mfma_f32_32x32x16_bf16 v[2:17], v[88:91], v[116:119], v[2:17]\n"
    "v_mfma_f32_32x32x16_bf16 v[2:17], v[92:95], v[120:123], v[2:17]\n"
    "v_mfma_f32_32x32x16_bf16 v[2:17], v[96:99], v[124:127], v[2:17]\n"
    "s_nop 11\n"
    "v_cvt_pk_bf16_f32 v2, v2, v3\n"
    "v_cvt_pk_bf16_f32 v3, v4, v5\n"
    "v_cvt_pk_bf16_f32 v4, v6, v7\n"
    "v_cvt_pk_bf16_f32 v5, v8, v9\n"
    "v_xor_b32_e32 v6, 64, v69\n"
    "ds_write_b128 v6, v[2:5]\n"
    "v_cvt_pk_bf16_f32 v6, v10, v11\n"
    "v_cvt_pk_bf16_f32 v7, v12, v13\n"
    "v_cvt_pk_bf16_f32 v8, v14, v15\n"
    "v_cvt_pk_bf16_f32 v9, v16, v17\n"
    "v_xor_b32_e32 v10, 0x50, v69\n"
    "ds_write_b128 v10, v[6:9]\n"
    "ds_read_b128 v[10:13], v70\n"
    "ds_read_b128 v[14:17], v70 offset:8192\n"
    "ds_read_b128 v[18:21], v70 offset:16384\n"
    "ds_read_b128 v[22:25], v70 offset:24576\n"
    "s_or_b32 s11, s11, 1\n"
    "v_add3_u32 v2, v26, v2, v3\n"
    "v_add3_u32 v2, v2, v4, v5\n"
    "v_add3_u32 v2, v2, v6, v7\n"
    "v_add3_u32 v2, v2, v8, v9\n"
    "v_mov_b32_e32 v3, v2\n"
    "s_nop 1\n"
    "v_permlane32_swap_b32_e32 v2, v3\n"
    "v_add3_u32 v2, v3, s11, v2\n"
    "buffer_atomic_add v2, v71, s[4:7], 0 offen sc1\n"
    "s_lshl_b32 s14, s9, 12\n"
    "s_lshl_b32 s15, s9, 11\n"
    "s_and_b32 s14, s14, 0x2000\n"
    "s_and_b32 s15, s15, 0x2000\n"
    "s_waitcnt lgkmcnt(3)\n"
    "buffer_store_dwordx4 v[10:13], v68, s[4:7], s14 offen sc1\n"
    "s_or_b32 s16, s15, 0x200000\n"
    "s_xor_b32 s14, s14, 0x2000\n"
    "s_waitcnt lgkmcnt(2)\n"
    "buffer_store_dwordx4 v[14:17], v68, s[4:7], s16 offen sc1\n"
    "s_waitcnt lgkmcnt(1)\n"
    "buffer_store_dwordx4 v[18:21], v68, s[4:7], s14 offen sc1\n"
    "s_xor_b32 s14, s15, 0x202000\n"
    "s_waitcnt lgkmcnt(0)\n"
    "buffer_store_dwordx4 v[22:25], v68, s[4:7], s14 offen sc1\n"
    "s_add_i32 s14, s10, 0x27420\n"
    "s_lshr_b32 s15, s2, 2\n"
    "v_mov_b32_e32 v2, s14\n"
    "s_and_b32 s14, s1, 15\n"
    "s_and_b32 s15, s15, 6\n"
    "s_lshl_b32 s16, s14, 4\n"
    "s_lshl_b32 s17, s15, 1\n"
    "s_lshr_b32 s3, s3, 7\n"
    "s_or_b32 s16, s17, s16\n"
    "s_add_i32 s3, s3, s16\n"
    "s_bfe_u32 s16, s2, 0x10002\n"
    "s_bfe_u32 s2, s2, 0x10001\n"
    "s_add_i32 s2, s2, 1\n"
    "s_lshr_b32 s2, s9, s2\n"
    "s_and_b32 s2, s2, 1\n"
    "s_xor_b32 s2, s2, s16\n"
    "s_lshl_b32 s3, s3, 14\n"
    "s_lshl_b32 s2, s2, 13\n"
    "s_or_b32 s2, s3, s2\n"
    "s_and_b32 s3, s8, 0x1000\n"
    "s_or_b32 s2, s2, s3\n"
    "s_add_i32 s2, s2, 0x208000\n"
    "v_or_b32_e32 v22, s2, v63\n"
    "s_and_b32 s2, s9, 1\n"
    "s_xor_b32 s2, s2, s16\n"
    "s_lshl_b32 s3, s15, 3\n"
    "v_mov_b32_e32 v3, 1\n"
    "s_lshl_b32 s2, s2, 13\n"
    "s_add_i32 s12, s12, s3\n"
    "s_lshl_b32 s3, s14, 2\n"
    "ds_write_b32 v2, v3\n"
    "v_add_u32_e32 v2, s12, v62\n"
    "s_or_b32 s2, s2, s3\n"
    "v_lshl_add_u32 v2, v2, 7, s2\n"
    "v_cmp_lt_u32_e32 vcc, 1, v62\n"
    "v_add_u32_e32 v2, 0x74400, v2\n"
    "v_lshlrev_b32_e32 v0, 5, v0\n"
    "v_cndmask_b32_e32 v3, 0, v67, vcc\n"
    "v_or_b32_e32 v23, v2, v3\n"
    "v_bitop3_b32 v2, s1, v64, 7 bitop3:0x6c\n"
    "v_lshlrev_b32_e32 v2, 4, v2\n"
    "v_lshl_or_b32 v2, s0, 7, v2\n"
    "v_and_or_b32 v3, v64, 3, s10\n"
    "v_add_u32_e32 v21, 0x22000, v2\n"
    "v_lshlrev_b32_e32 v2, 8, v65\n"
    "v_lshlrev_b32_e32 v3, 4, v3\n"
    "v_and_or_b32 v2, v66, s13, v2\n"
    "v_xor_b32_e32 v0, v3, v0\n"
    "s_movk_i32 s0, 0xf0\n"
    "v_and_or_b32 v0, v0, s0, v2\n"
    "v_lshlrev_b32_e32 v1, 3, v1\n"
    "s_mov_b32 s0, 0x25000\n"
    "v_mov_b32_e32 v20, 0x27400\n"
    "v_or3_b32 v24, v0, v1, s0\n"
    "v_mov_b32_e32 v0, 0xfffff\n"
    ".Lfd_R_AM_3:\n"
    "ds_read_b64 v[6:7], v20 offset:24\n"
    "ds_read_b128 v[2:5], v20 offset:32\n"
    "v_subrev_co_u32_e32 v0, vcc, 1, v0\n"
    "s_waitcnt lgkmcnt(0)\n"
    "v_bitop3_b32 v1, v2, v3, v6 bitop3:0x80\n"
    "v_bitop3_b32 v1, v1, v5, v4 bitop3:0x80\n"
    "s_nop 0\n"
    "v_readfirstlane_b32 s0, v1\n"
    "s_cmp_lg_u32 s0, 0\n"
    "s_cselect_b64 s[0:1], -1, 0\n"
    "s_or_b64 s[0:1], s[0:1], vcc\n"
    "s_andn2_b64 vcc, exec, s[0:1]\n"
    "v_readfirstlane_b32 s0, v7\n"
    "s_cbranch_vccnz .Lfd_R_AM_3\n"
    ".Lfd_R_AM_4:\n"
    "s_memrealtime s[2:3]\n"
    "s_waitcnt lgkmcnt(0)\n"
    "s_sub_i32 s1, s2, s0\n"
    "s_addk_i32 s1, 0x80\n"
    "s_cmpk_lt_u32 s1, 0x157\n"
    "s_cbranch_scc1 .Lfd_R_AM_4\n"
    "buffer_load_dwordx4 v[16:19], v22, s[4:7], 0 offen sc1\n"
    "buffer_load_dwordx4 v[12:15], v22, s[4:7], 0 offen offset:1024 sc1\n"
    "v_add_u32_e32 v25, 0x400, v22\n"
    "buffer_load_dwordx4 v[8:11], v22, s[4:7], 0 offen offset:2048 sc1\n"
    "v_add_u32_e32 v26, 0x800, v22\n"
    "buffer_load_dword v33, v23, s[4:7], 0 offen sc1\n"
    "buffer_load_dwordx4 v[4:7], v22, s[4:7], 0 offen offset:3072 sc1\n"
    "v_add_u32_e32 v27, 0xc00, v22\n"
    "v_mov_b32_e32 v28, 0xfffff\n"
    ".Lfd_R_AM_6:\n"
    "ds_read_b32 v29, v20 offset:4\n"
    "s_waitcnt lgkmcnt(1)\n"
    "ds_read_b128 v[0:3], v21\n"
    "v_subrev_co_u32_e32 v28, vcc, 1, v28\n"
    "s_waitcnt lgkmcnt(1)\n"
    "v_readfirstlane_b32 s0, v29\n"
    "s_cmp_lg_u32 s0, 0\n"
    "s_cselect_b64 s[0:1], -1, 0\n"
    "s_or_b64 s[0:1], s[0:1], vcc\n"
    "s_andn2_b64 vcc, exec, s[0:1]\n"
    "s_cbranch_vccnz .Lfd_R_AM_6\n"
    "s_lshl_b32 s0, s11, 5\n"
    "s_waitcnt lgkmcnt(0)\n"
    "v_lshlrev_b32_e32 v28, 16, v0\n"
    "v_lshlrev_b32_e32 v29, 16, v1\n"
    "v_lshlrev_b32_e32 v30, 16, v2\n"
    "v_lshlrev_b32_e32 v31, 16, v3\n"
    "s_sub_i32 s2, 2, s0\n"
    "v_mov_b32_e32 v32, 0x8000\n"
    "s_branch .Lfd_R_AM_9\n"
    ".Lfd_R_AM_8:\n"
    "s_andn2_b64 vcc, exec, s[0:1]\n"
    "s_cbranch_vccz .Lfd_R_AM_11\n"
    ".Lfd_R_AM_9:\n"
    "v_mov_b32_e32 v21, 0\n"
    "v_mov_b32_e32 v34, 0\n"
    "s_waitcnt vmcnt(4)\n"
    "v_add_u32_e32 v20, v16, v17\n"
    "v_dot2c_f32_bf16_e32 v21, v16, v0\n"
    "v_dot2c_f32_bf16_e32 v34, v16, v28\n"
    "v_mov_b32_e32 v16, 0\n"
    "v_mov_b32_e32 v35, 0\n"
    "v_dot2c_f32_bf16_e32 v16, v17, v0\n"
    "v_dot2c_f32_bf16_e32 v35, v17, v28\n"
    "v_mov_b32_e32 v17, 0\n"
    "v_mov_b32_e32 v36, 0\n"
    "v_add3_u32 v20, v20, v18, v19\n"
    "v_dot2c_f32_bf16_e32 v17, v18, v0\n"
    "v_dot2c_f32_bf16_e32 v36, v18, v28\n"
    "v_mov_b32_e32 v18, 0\n"
    "v_mov_b32_e32 v37, 0\n"
    "v_dot2c_f32_bf16_e32 v18, v19, v0\n"
    "v_dot2c_f32_bf16_e32 v37, v19, v28\n"
    "s_waitcnt vmcnt(3)\n"
    "v_add3_u32 v19, v20, v12, v13\n"
    "v_add3_u32 v19, v19, v14, v15\n"
    "v_dot2c_f32_bf16_e32 v21, v12, v1\n"
    "v_dot2c_f32_bf16_e32 v34, v12, v29\n"
    "v_dot2c_f32_bf16_e32 v16, v13, v1\n"
    "v_dot2c_f32_bf16_e32 v35, v13, v29\n"
    "v_dot2c_f32_bf16_e32 v17, v14, v1\n"
    "v_dot2c_f32_bf16_e32 v36, v14, v29\n"
    "v_dot2c_f32_bf16_e32 v18, v15, v1\n"
    "v_dot2c_f32_bf16_e32 v37, v15, v29\n"
    "s_waitcnt vmcnt(2)\n"
    "v_add3_u32 v12, v19, v8, v9\n"
    "v_add3_u32 v12, v12, v10, v11\n"
    "v_dot2c_f32_bf16_e32 v21, v8, v2\n"
    "v_dot2c_f32_bf16_e32 v34, v8, v30\n"
    "v_dot2c_f32_bf16_e32 v16, v9, v2\n"
    "v_dot2c_f32_bf16_e32 v35, v9, v30\n"
    "v_dot2c_f32_bf16_e32 v17, v10, v2\n"
    "v_dot2c_f32_bf16_e32 v36, v10, v30\n"
    "v_dot2c_f32_bf16_e32 v18, v11, v2\n"
    "v_dot2c_f32_bf16_e32 v37, v11, v30\n"
    "s_waitcnt vmcnt(1)\n"
    "v_sub_u32_e32 v8, v12, v33\n"
    "s_waitcnt vmcnt(0)\n"
    "v_add3_u32 v8, v8, v4, v5\n"
    "v_add3_u32 v8, v8, v6, v7\n"
    "v_dot2c_f32_bf16_e32 v21, v4, v3\n"
    "v_dot2c_f32_bf16_e32 v34, v4, v31\n"
    "v_add_u32_dpp v8, v8, v8 row_ror:8 row_mask:0xf bank_mask:0xf bound_ctrl:1\n"
    "v_dot2c_f32_bf16_e32 v16, v5, v3\n"
    "v_dot2c_f32_bf16_e32 v35, v5, v31\n"
    "v_add_u32_dpp v8, v8, v8 row_ror:4 row_mask:0xf bank_mask:0xf bound_ctrl:1\n"
    "v_dot2c_f32_bf16_e32 v17, v6, v3\n"
    "v_dot2c_f32_bf16_e32 v36, v6, v31\n"
    "v_add_u32_dpp v8, v8, v8 row_ror:2 row_mask:0xf bank_mask:0xf bound_ctrl:1\n"
    "v_dot2c_f32_bf16_e32 v18, v7, v3\n"
    "v_dot2c_f32_bf16_e32 v37, v7, v31\n"
    "v_add_u32_dpp v8, v8, v8 row_ror:1 row_mask:0xf bank_mask:0xf bound_ctrl:1\n"
    "s_nop 0\n"
    "v_readlane_b32 s0, v8, 0\n"
    "v_readlane_b32 s1, v8, 16\n"
    "s_add_i32 s0, s1, s0\n"
    "v_readlane_b32 s1, v8, 32\n"
    "v_permlane32_swap_b32_e32 v21, v17\n"
    "v_permlane32_swap_b32_e32 v34, v36\n"
    "s_add_i32 s0, s0, s1\n"
    "v_readlane_b32 s1, v8, 48\n"
    "v_add_f32_e32 v4, v21, v17\n"
    "v_add_f32_e32 v5, v34, v36\n"
    "v_permlane32_swap_b32_e32 v16, v18\n"
    "v_permlane32_swap_b32_e32 v35, v37\n"
    "s_add_i32 s0, s0, s1\n"
    "v_cvt_pk_bf16_f32 v20, v4, v5\n"
    "v_add_f32_e32 v4, v16, v18\n"
    "v_add_f32_e32 v5, v35, v37\n"
    "v_cvt_pk_bf16_f32 v21, v4, v5\n"
    "s_cmp_eq_u32 s0, s2\n"
    "s_mov_b64 s[0:1], -1\n"
    "s_cbranch_scc1 .Lfd_R_AM_8\n"
    "buffer_load_dwordx4 v[16:19], v22, s[4:7], 0 offen sc1\n"
    "buffer_load_dwordx4 v[12:15], v25, s[4:7], 0 offen sc1\n"
    "buffer_load_dwordx4 v[8:11], v26, s[4:7], 0 offen sc1\n"
    "buffer_load_dword v33, v23, s[4:7], 0 offen sc1\n"
    "buffer_load_dwordx4 v[4:7], v27, s[4:7], 0 offen sc1\n"
    "v_add_u32_e32 v32, -1, v32\n"
    "v_cmp_eq_u32_e64 s[0:1], 0, v32\n"
    "s_branch .Lfd_R_AM_8\n"
    ".Lfd_R_AM_11:\n"
    "ds_write_b64 v24, v[20:21]\n"
    "s_endpgm\n"
    ".p2align 7\n"
    ".Lfd_R_AS:\n"
    "s_load_dwordx4 s[4:7], s[0:1], 0x30\n"
    "v_readfirstlane_b32 s0, v0\n"
    "v_and_b32_e32 v1, 63, v0\n"
    "s_bfe_u32 s13, s2, 0x20003\n"
    "s_bfe_u32 s14, s0, 0x20006\n"
    "s_and_b32 s1, s2, 7\n"
    "s_lshl_b32 s12, s13, 7\n"
    "s_lshl_b32 s15, s14, 5\n"
    "v_bfe_u32 v5, v1, 4, 1\n"
    "v_lshrrev_b32_e32 v7, 3, v0\n"
    "s_waitcnt lgkmcnt(0)\n"
    "s_mov_b64 s[8:9], s[6:7]\n"
    "s_lshl_b32 s1, s1, 2\n"
    "s_bfe_u32 s6, s2, 0x20005\n"
    "s_or_b32 s12, s15, s12\n"
    "v_lshlrev_b32_e32 v6, 4, v5\n"
    "v_and_b32_e32 v7, 4, v7\n"
    "v_and_b32_e32 v3, 15, v0\n"
    "s_or_b32 s1, s1, s6\n"
    "v_or3_b32 v6, s12, v6, v7\n"
    "v_lshlrev_b32_e32 v6, 13, v6\n"
    "s_lshl_b32 s12, s1, 8\n"
    "v_lshlrev_b32_e32 v7, 4, v3\n"
    "s_mov_b32 s7, 0x20000\n"
    "v_or3_b32 v21, v6, s12, v7\n"
    "s_mov_b32 s10, 0x400000\n"
    "s_and_b32 s9, s9, 0xffff\n"
    "s_mov_b32 s11, s7\n"
    "s_barrier\n"
    "v_bfe_u32 v4, v0, 5, 1\n"
    "s_barrier\n"
    "v_mov_b32_e32 v6, 0x27fd4\n"
    "v_mov_b32_e32 v8, 0x27ff0\n"
    "v_lshlrev_b32_e32 v9, 8, v0\n"
    "v_and_b32_e32 v10, 1, v0\n"
    "ds_read_b32 v6, v6\n"
    "ds_read_b32 v28, v8\n"
    "v_lshlrev_b32_e32 v8, 12, v4\n"
    "v_and_b32_e32 v9, 0xc00, v9\n"
    "v_lshlrev_b32_e32 v11, 3, v10\n"
    "v_or3_b32 v8, v8, v9, v11\n"
    "v_bfe_u32 v9, v0, 3, 3\n"
    "v_and_b32_e32 v11, 2, v9\n"
    "v_bfe_u32 v12, v0, 1, 1\n"
    "v_and_or_b32 v13, v0, 12, v4\n"
    "v_bitop3_b32 v11, v11, v13, v12 bitop3:0x36\n"
    "s_lshl_b32 s15, s14, 8\n"
    "v_lshlrev_b32_e32 v11, 4, v11\n"
    "v_or3_b32 v8, v11, v8, s15\n"
    "s_add_i32 s16, s2, 0xffffff80\n"
    "v_xor_b32_e32 v11, 32, v8\n"
    "v_mov_b32_e32 v17, v11\n"
    "v_add_u32_e32 v78, 0x10000, v11\n"
    "v_xor_b32_e32 v11, 64, v8\n"
    "s_lshl_b32 s19, s16, 3\n"
    "v_bfe_u32 v2, v0, 4, 2\n"
    "v_mov_b32_e32 v20, v11\n"
    "v_add_u32_e32 v77, 0x10000, v11\n"
    "v_mov_b32_e32 v11, 0x74000\n"
    "s_and_b32 s19, s19, 0xffffff80\n"
    "v_lshl_or_b32 v11, v2, 13, v11\n"
    "s_lshl_b32 s18, s14, 11\n"
    "v_lshl_or_b32 v3, v3, 2, s19\n"
    "v_mov_b32_e32 v18, v8\n"
    "v_add_u32_e32 v76, 0x10000, v8\n"
    "v_xor_b32_e32 v8, 0x60, v8\n"
    "v_add3_u32 v3, v3, v11, s18\n"
    "v_mov_b32_e32 v11, 0x7ff00000\n"
    "v_cmp_lt_u32_e32 vcc, 31, v1\n"
    "v_mov_b32_e32 v19, v8\n"
    "v_add_u32_e32 v79, 0x10000, v8\n"
    "v_and_b32_e32 v8, 31, v0\n"
    "v_cndmask_b32_e32 v13, 0, v11, vcc\n"
    "v_or_b32_e32 v22, v3, v13\n"
    "v_lshlrev_b32_e32 v3, 10, v8\n"
    "v_lshl_or_b32 v3, v12, 7, v3\n"
    "v_and_b32_e32 v12, 7, v0\n"
    "v_lshlrev_b32_e32 v13, 4, v12\n"
    "v_bitop3_b32 v14, v13, v0, 32 bitop3:0x78\n"
    "v_or3_b32 v75, v3, v14, s15\n"
    "v_lshlrev_b32_e32 v3, 10, v9\n"
    "v_lshl_or_b32 v3, v5, 7, v3\n"
    "v_or3_b32 v74, v3, v13, s15\n"
    "s_lshl_b32 s15, s16, 8\n"
    "s_and_b32 s15, s15, 0xffffc000\n"
    "v_bitop3_b32 v5, v9, s2, 63 bitop3:0x78\n"
    "v_bitop3_b32 v13, v9, v0, 7 bitop3:0x78\n"
    "s_add_i32 s15, s15, 0x200000\n"
    "v_lshlrev_b32_e32 v5, 7, v5\n"
    "v_lshlrev_b32_e32 v13, 4, v13\n"
    "v_or3_b32 v5, s15, v5, v13\n"
    "s_and_b32 s15, s1, 15\n"
    "s_lshr_b32 s3, s0, 6\n"
    "s_lshl_b32 s16, s15, 4\n"
    "s_lshl_b32 s19, s13, 2\n"
    "s_or_b32 s16, s16, s19\n"
    "s_bfe_u32 s19, s3, 0x10001\n"
    "s_or_b32 s16, s19, s16\n"
    "s_bfe_u32 s19, s2, 0x10002\n"
    "s_bfe_u32 s2, s2, 0x10001\n"
    "s_waitcnt lgkmcnt(1)\n"
    "v_readfirstlane_b32 s12, v6\n"
    "s_add_i32 s20, s2, 1\n"
    "s_lshr_b32 s20, s12, s20\n"
    "s_and_b32 s20, s20, 1\n"
    "s_xor_b32 s20, s20, s19\n"
    "s_lshl_b32 s16, s16, 14\n"
    "s_lshl_b32 s20, s20, 13\n"
    "s_lshl_b32 s0, s0, 6\n"
    "s_or_b32 s16, s16, s20\n"
    "s_and_b32 s0, s0, 0x1000\n"
    "s_or_b32 s16, s16, s0\n"
    "v_lshlrev_b32_e32 v6, 4, v1\n"
    "s_add_i32 s16, s16, 0x200000\n"
    "v_or_b32_e32 v68, s16, v6\n"
    "s_and_b32 s16, s12, 1\n"
    "s_lshl_b32 s17, s14, 1\n"
    "s_xor_b32 s16, s16, s19\n"
    "s_lshl_b32 s19, s13, 4\n"
    "v_and_or_b32 v3, v0, 56, s17\n"
    "s_lshl_b32 s16, s16, 13\n"
    "s_or_b32 s17, s17, s19\n"
    "s_lshl_b32 s15, s15, 2\n"
    "v_lshl_add_u32 v73, v3, 15, v5\n"
    "v_add_u32_e32 v3, s17, v1\n"
    "s_or_b32 s15, s16, s15\n"
    "v_lshl_add_u32 v3, v3, 7, s15\n"
    "v_cmp_lt_u32_e32 vcc, 1, v1\n"
    "v_add_u32_e32 v3, 0x74000, v3\n"
    "s_and_b32 s15, s1, 7\n"
    "v_cndmask_b32_e32 v5, 0, v11, vcc\n"
    "v_or_b32_e32 v69, v3, v5\n"
    "v_bitop3_b32 v3, s1, v9, 7 bitop3:0x6c\n"
    "v_lshlrev_b32_e32 v3, 4, v3\n"
    "v_lshl_or_b32 v3, s14, 7, v3\n"
    "v_or_b32_e32 v72, 0x22000, v3\n"
    "v_lshlrev_b32_e32 v3, 9, v0\n"
    "v_lshlrev_b32_e32 v0, 1, v0\n"
    "v_lshlrev_b32_e32 v9, 5, v12\n"
    "s_lshl_b32 s1, s14, 6\n"
    "v_and_b32_e32 v0, 48, v0\n"
    "v_and_b32_e32 v3, 0xc00, v3\n"
    "v_lshlrev_b32_e32 v5, 8, v10\n"
    "v_bitop3_b32 v0, s1, v9, v0 bitop3:0x36\n"
    "v_or3_b32 v0, v3, v5, v0\n"
    "v_lshlrev_b32_e32 v3, 3, v4\n"
    "s_mov_b32 s1, 0x24000\n"
    "v_or3_b32 v71, v0, v3, s1\n"
    "v_lshlrev_b32_e32 v0, 10, v4\n"
    "v_lshlrev_b32_e32 v3, 4, v8\n"
    "s_lshl_b32 s1, s2, 14\n"
    "s_lshl_b32 s2, s15, 10\n"
    "s_lshl_b32 s13, s13, 8\n"
    "v_or3_b32 v0, v0, v3, s18\n"
    "s_or_b32 s2, s2, s13\n"
    "v_or_b32_e32 v70, 0x24000, v0\n"
    "v_mov_b32_e32 v0, 0x26000\n"
    "s_or_b32 s1, s2, s1\n"
    "v_lshl_or_b32 v66, v1, 3, v0\n"
    "v_lshl_or_b32 v0, v2, 2, s1\n"
    "s_lshl_b32 s1, s3, 2\n"
    "s_add_i32 s1, s1, -16\n"
    "s_lshl_b32 s2, s1, 11\n"
    "s_and_b32 s2, s2, 0xffffc000\n"
    "s_or_b32 s0, s2, s0\n"
    "s_or_b32 s0, s0, s16\n"
    "s_add_i32 s0, s0, 0x58000\n"
    "v_or3_b32 v0, s16, v0, v7\n"
    "v_or_b32_e32 v23, s0, v6\n"
    "s_lshl_b32 s0, s1, 10\n"
    "v_add_u32_e32 v65, 0x78000, v0\n"
    "s_add_i32 s0, s0, 0x20000\n"
    "v_bitop3_b32 v0, s3, v1, 3 bitop3:0x6c\n"
    "v_or_b32_e32 v16, 0x20000, v6\n"
    "v_mov_b32_e32 v64, 0x27400\n"
    "v_lshl_or_b32 v67, s14, 10, v66\n"
    "v_lshl_or_b32 v24, v0, 4, s0\n"
    "s_mov_b32 s6, 0x600000\n"
    "s_and_b32 s5, s5, 0xffff\n"
    "s_barrier\n"
    "buffer_load_dwordx4 v[0:3], v23, s[4:7], 0 offen sc1\n"
    "s_mov_b32 s13, 0x10000\n"
    "buffer_load_dwordx4 v[4:7], v23, s[4:7], 0 offen offset:1024 sc1\n"
    "buffer_load_dwordx4 v[8:11], v23, s[4:7], 0 offen offset:2048 sc1\n"
    "buffer_load_dwordx4 v[12:15], v23, s[4:7], 0 offen offset:3072 sc1\n"
    "s_waitcnt vmcnt(3)\n"
    "v_max_u32_e32 v25, v0, v1\n"
    "v_max3_u32 v25, v25, v2, v3\n"
    "ds_write_b128 v24, v[0:3]\n"
    "s_waitcnt vmcnt(2)\n"
    "v_max3_u32 v0, v25, v4, v5\n"
    "v_xor_b32_e32 v25, 64, v24\n"
    "v_max3_u32 v0, v0, v6, v7\n"
    "ds_write_b128 v25, v[4:7] offset:1024\n"
    "s_waitcnt vmcnt(1)\n"
    "v_max3_u32 v0, v0, v8, v9\n"
    "v_xor_b32_e32 v26, 0x80, v24\n"
    "v_max3_u32 v0, v0, v10, v11\n"
    "ds_write_b128 v26, v[8:11] offset:2048\n"
    "s_waitcnt vmcnt(0)\n"
    "v_max3_u32 v0, v0, v12, v13\n"
    "v_xor_b32_e32 v27, 0xc0, v24\n"
    "v_max3_u32 v0, v0, v14, v15\n"
    "ds_write_b128 v27, v[12:15] offset:3072\n"
    "v_cmp_ne_u32_e32 vcc, -1, v0\n"
    "s_cmp_eq_u64 vcc, exec\n"
    "s_waitcnt lgkmcnt(4)\n"
    "v_readfirstlane_b32 s2, v28\n"
    "s_cbranch_scc1 .Lfd_R_AS_4\n"
    "v_add_u32_e32 v28, 0x400, v23\n"
    "v_add_u32_e32 v29, 0x800, v23\n"
    "v_add_u32_e32 v30, 0xc00, v23\n"
    "v_mov_b32_e32 v31, 0x7fff\n"
    ".Lfd_R_AS_2:\n"
    "buffer_load_dwordx4 v[0:3], v23, s[4:7], 0 offen sc1\n"
    "buffer_load_dwordx4 v[4:7], v28, s[4:7], 0 offen sc1\n"
    "buffer_load_dwordx4 v[8:11], v29, s[4:7], 0 offen sc1\n"
    "buffer_load_dwordx4 v[12:15], v30, s[4:7], 0 offen sc1\n"
    "v_subrev_co_u32_e32 v31, vcc, 1, v31\n"
    "s_waitcnt vmcnt(3)\n"
    "v_max_u32_e32 v32, v0, v1\n"
    "v_max3_u32 v32, v32, v2, v3\n"
    "s_waitcnt vmcnt(2)\n"
    "v_max3_u32 v32, v32, v4, v5\n"
    "v_max3_u32 v32, v32, v6, v7\n"
    "s_waitcnt vmcnt(1)\n"
    "v_max3_u32 v32, v32, v8, v9\n"
    "v_max3_u32 v32, v32, v10, v11\n"
    "s_waitcnt vmcnt(0)\n"
    "v_max3_u32 v32, v32, v12, v13\n"
    "v_max3_u32 v32, v32, v14, v15\n"
    "v_cmp_ne_u32_e64 s[0:1], -1, v32\n"
    "s_cmp_eq_u64 s[0:1], exec\n"
    "s_cselect_b64 s[0:1], -1, 0\n"
    "s_or_b64 s[0:1], s[0:1], vcc\n"
    "s_andn2_b64 vcc, exec, s[0:1]\n"
    "s_cbranch_vccnz .Lfd_R_AS_2\n"
    "ds_write_b128 v24, v[0:3]\n"
    "ds_write_b128 v25, v[4:7] offset:1024\n"
    "ds_write_b128 v26, v[8:11] offset:2048\n"
    "ds_write_b128 v27, v[12:15] offset:3072\n"
    ".Lfd_R_AS_4:\n"
    "s_movk_i32 s0, 0x2000\n"
    "s_waitcnt lgkmcnt(0)\n"
    "s_barrier\n"
    "s_sleep 2\n"
    "s_movk_i32 s1, 0x4000\n"
    "buffer_load_dwordx4 v[32:35], v21, s[8:11], s0 offen nt\n"
    "buffer_load_dwordx4 v[36:39], v21, s[8:11], s1 offen nt\n"
    "s_movk_i32 s0, 0x6000\n"
    "buffer_load_dwordx4 v[40:43], v21, s[8:11], 0 offen nt\n"
    "buffer_load_dwordx4 v[44:47], v21, s[8:11], s13 offen nt\n"
    "s_mov_b32 s1, 0x12000\n"
    "buffer_load_dwordx4 v[48:51], v21, s[8:11], s0 offen nt\n"
    "buffer_load_dwordx4 v[52:55], v21, s[8:11], s1 offen nt\n"
    "s_mov_b32 s0, 0x14000\n"
    "s_mov_b32 s1, 0x16000\n"
    "buffer_load_dwordx4 v[56:59], v21, s[8:11], s0 offen nt\n"
    "buffer_load_dwordx4 v[60:63], v21, s[8:11], s1 offen nt\n"
    "s_barrier\n"
    "ds_read_b64_tr_b16 v[0:1], v18\n"
    "ds_read_b64_tr_b16 v[80:81], v18 offset:16384\n"
    "ds_read_b64_tr_b16 v[84:85], v18 offset:32768\n"
    "ds_read_b64_tr_b16 v[88:89], v18 offset:49152\n"
    "ds_read_b64_tr_b16 v[2:3], v17 offset:8192\n"
    "ds_read_b64_tr_b16 v[82:83], v17 offset:24576\n"
    "ds_read_b64_tr_b16 v[86:87], v17 offset:40960\n"
    "ds_read_b64_tr_b16 v[90:91], v17 offset:57344\n"
    "ds_read_b64_tr_b16 v[4:5], v20\n"
    "ds_read_b64_tr_b16 v[92:93], v20 offset:16384\n"
    "ds_read_b64_tr_b16 v[96:97], v20 offset:32768\n"
    "ds_read_b64_tr_b16 v[100:101], v20 offset:49152\n"
    "ds_read_b64_tr_b16 v[6:7], v19 offset:8192\n"
    "ds_read_b64_tr_b16 v[94:95], v19 offset:24576\n"
    "ds_read_b64_tr_b16 v[98:99], v19 offset:40960\n"
    "ds_read_b64_tr_b16 v[102:103], v19 offset:57344\n"
    "s_setprio 3\n"
    "s_lshl_b32 s0, s12, 13\n"
    "v_mov_b32_e32 v8, 0x2000\n"
    "v_bitop3_b32 v144, v22, s0, v8 bitop3:0x78\n"
    "s_barrier\n"
    "s_waitcnt lgkmcnt(0)\n"
    "s_barrier\n"
    "ds_read_b128 v[8:11], v16\n"
    "ds_read_b128 v[104:107], v16 offset:1024\n"
    "ds_read_b128 v[108:111], v16 offset:2048\n"
    "ds_read_b128 v[112:115], v16 offset:3072\n"
    "ds_read_b128 v[116:119], v16 offset:4096\n"
    "ds_read_b128 v[120:123], v16 offset:5120\n"
    "ds_read_b128 v[124:127], v16 offset:6144\n"
    "ds_read_b128 v[128:131], v16 offset:7168\n"
    "s_waitcnt lgkmcnt(7)\n"
    "v_mfma_f32_32x32x16_bf16 v[16:31], v[0:3], v[8:11], 0\n"
    "ds_read_b64_tr_b16 v[132:133], v76\n"
    "ds_read_b64_tr_b16 v[134:135], v78 offset:8192\n"
    "v_mfma_f32_32x32x16_bf16 v[0:15], v[4:7], v[8:11], 0\n"
    "ds_read_b64_tr_b16 v[136:137], v77\n"
    "ds_read_b64_tr_b16 v[138:139], v79 offset:8192\n"
    "s_waitcnt lgkmcnt(10)\n"
    "v_mfma_f32_32x32x16_bf16 v[16:31], v[80:83], v[104:107], v[16:31]\n"
    "ds_read_b64_tr_b16 v[140:141], v76 offset:16384\n"
    "ds_read_b64_tr_b16 v[142:143], v78 offset:24576\n"
    "v_mfma_f32_32x32x16_bf16 v[0:15], v[92:95], v[104:107], v[0:15]\n"
    "ds_read_b64_tr_b16 v[80:81], v77 offset:16384\n"
    "ds_read_b64_tr_b16 v[82:83], v79 offset:24576\n"
    "s_waitcnt lgkmcnt(13)\n"
    "v_mfma_f32_32x32x16_bf16 v[16:31], v[84:87], v[108:111], v[16:31]\n"
    "ds_read_b64_tr_b16 v[92:93], v76 offset:32768\n"
    "ds_read_b64_tr_b16 v[94:95], v78 offset:40960\n"
    "v_mfma_f32_32x32x16_bf16 v[0:15], v[96:99], v[108:111], v[0:15]\n"
    "ds_read_b64_tr_b16 v[84:85], v77 offset:32768\n"
    "ds_read_b64_tr_b16 v[86:87], v79 offset:40960\n"
    "s_waitcnt lgkmcnt(14)\n"
    "v_mfma_f32_32x32x16_bf16 v[16:31], v[88:91], v[112:115], v[16:31]\n"
    "ds_read_b64_tr_b16 v[96:97], v76 offset:49152\n"
    "ds_read_b64_tr_b16 v[98:99], v78 offset:57344\n"
    "v_mfma_f32_32x32x16_bf16 v[0:15], v[100:103], v[112:115], v[0:15]\n"
    "ds_read_b64_tr_b16 v[76:77], v77 offset:49152\n"
    "ds_read_b64_tr_b16 v[78:79], v79 offset:57344\n"
    "s_waitcnt lgkmcnt(14)\n"
    "v_mfma_f32_32x32x16_bf16 v[16:31], v[132:135], v[116:119], v[16:31]\n"
    "s_waitcnt lgkmcnt(12)\n"
    "v_mfma_f32_32x32x16_bf16 v[0:15], v[136:139], v[116:119], v[0:15]\n"
    "s_waitcnt lgkmcnt(10)\n"
    "v_mfma_f32_32x32x16_bf16 v[16:31], v[140:143], v[120:123], v[16:31]\n"
    "s_waitcnt lgkmcnt(8)\n"
    "v_mfma_f32_32x32x16_bf16 v[0:15], v[80:83], v[120:123], v[0:15]\n"
    "s_waitcnt lgkmcnt(6)\n"
    "v_mfma_f32_32x32x16_bf16 v[16:31], v[92:95], v[124:127], v[16:31]\n"
    "s_waitcnt lgkmcnt(4)\n"
    "v_mfma_f32_32x32x16_bf16 v[0:15], v[84:87], v[124:127], v[0:15]\n"
    "s_waitcnt lgkmcnt(2)\n"
    "v_mfma_f32_32x32x16_bf16 v[16:31], v[96:99], v[128:131], v[16:31]\n"
    "s_waitcnt lgkmcnt(0)\n"
    "v_mfma_f32_32x32x16_bf16 v[0:15], v[76:79], v[128:131], v[0:15]\n"
    "s_setprio 1\n"
    "s_nop 8\n"
    "v_cvt_pk_bf16_f32 v16, v16, v17\n"
    "v_cvt_pk_bf16_f32 v17, v18, v19\n"
    "v_cvt_pk_bf16_f32 v18, v20, v21\n"
    "v_cvt_pk_bf16_f32 v19, v22, v23\n"
    "v_cvt_pk_bf16_f32 v20, v24, v25\n"
    "v_cvt_pk_bf16_f32 v21, v26, v27\n"
    "v_cvt_pk_bf16_f32 v22, v28, v29\n"
    "v_cvt_pk_bf16_f32 v23, v30, v31\n"
    "v_xor_b32_e32 v24, 16, v75\n"
    "v_cvt_pk_bf16_f32 v0, v0, v1\n"
    "v_cvt_pk_bf16_f32 v1, v2, v3\n"
    "v_cvt_pk_bf16_f32 v2, v4, v5\n"
    "v_cvt_pk_bf16_f32 v3, v6, v7\n"
    "v_xor_b32_e32 v4, 64, v75\n"
    "ds_write_b128 v75, v[16:19]\n"
    "ds_write_b128 v24, v[20:23]\n"
    "ds_write_b128 v4, v[0:3]\n"
    "v_cvt_pk_bf16_f32 v4, v8, v9\n"
    "v_cvt_pk_bf16_f32 v5, v10, v11\n"
    "v_cvt_pk_bf16_f32 v6, v12, v13\n"
    "v_cvt_pk_bf16_f32 v7, v14, v15\n"
    "v_xor_b32_e32 v8, 0x50, v75\n"
    "ds_write_b128 v8, v[4:7]\n"
    "ds_read_b128 v[8:11], v74\n"
    "ds_read_b128 v[12:15], v74 offset:8192\n"
    "ds_read_b128 v[24:27], v74 offset:16384\n"
    "ds_read_b128 v[28:31], v74 offset:24576\n"
    "v_add_u32_e32 v16, v16, v17\n"
    "v_add3_u32 v16, v16, v18, v19\n"
    "v_add3_u32 v16, v16, v20, v21\n"
    "s_or_b32 s0, s2, 1\n"
    "v_add3_u32 v16, v16, v22, v23\n"
    "v_add3_u32 v0, v16, v0, v1\n"
    "v_add3_u32 v0, v0, v2, v3\n"
    "v_add3_u32 v0, v0, v4, v5\n"
    "v_add3_u32 v0, v0, v6, v7\n"
    "v_mov_b32_e32 v1, v0\n"
    "s_nop 1\n"
    "v_permlane32_swap_b32_e32 v0, v1\n"
    "v_add3_u32 v0, v1, s0, v0\n"
    "buffer_atomic_add v0, v144, s[4:7], 0 offen sc1\n"
    "s_lshl_b32 s1, s12, 12\n"
    "s_lshl_b32 s2, s12, 11\n"
    "s_and_b32 s1, s1, 0x2000\n"
    "s_and_b32 s2, s2, 0x2000\n"
    "s_waitcnt lgkmcnt(3)\n"
    "buffer_store_dwordx4 v[8:11], v73, s[4:7], s1 offen sc1\n"
    "s_or_b32 s8, s2, 0x200000\n"
    "s_xor_b32 s1, s1, 0x2000\n"
    "s_waitcnt lgkmcnt(2)\n"
    "buffer_store_dwordx4 v[12:15], v73, s[4:7], s8 offen sc1\n"
    "s_waitcnt lgkmcnt(1)\n"
    "buffer_store_dwordx4 v[24:27], v73, s[4:7], s1 offen sc1\n"
    "s_xor_b32 s1, s2, 0x202000\n"
    "s_waitcnt lgkmcnt(0)\n"
    "buffer_store_dwordx4 v[28:31], v73, s[4:7], s1 offen sc1\n"
    "s_setprio 1\n"
    "s_mov_b32 s1, 0x5040100\n"
    "s_mov_b32 s2, 0x7060302\n"
    "s_waitcnt vmcnt(9)\n"
    "v_perm_b32 v4, v32, v40, s1\n"
    "s_waitcnt vmcnt(7)\n"
    "v_perm_b32 v5, v48, v36, s1\n"
    "s_waitcnt vmcnt(6)\n"
    "v_perm_b32 v6, v52, v44, s1\n"
    "s_waitcnt vmcnt(4)\n"
    "v_perm_b32 v7, v60, v56, s1\n"
    "v_perm_b32 v0, v32, v40, s2\n"
    "v_perm_b32 v1, v48, v36, s2\n"
    "v_perm_b32 v2, v52, v44, s2\n"
    "v_perm_b32 v3, v60, v56, s2\n"
    "v_perm_b32 v12, v33, v41, s1\n"
    "v_perm_b32 v13, v49, v37, s1\n"
    "v_perm_b32 v14, v53, v45, s1\n"
    "v_perm_b32 v15, v61, v57, s1\n"
    "v_perm_b32 v8, v33, v41, s2\n"
    "v_perm_b32 v9, v49, v37, s2\n"
    "v_perm_b32 v10, v53, v45, s2\n"
    "v_perm_b32 v11, v61, v57, s2\n"
    "v_perm_b32 v24, v34, v42, s1\n"
    "v_perm_b32 v25, v50, v38, s1\n"
    "v_perm_b32 v26, v54, v46, s1\n"
    "v_perm_b32 v27, v62, v58, s1\n"
    "v_perm_b32 v16, v34, v42, s2\n"
    "v_perm_b32 v17, v50, v38, s2\n"
    "v_perm_b32 v18, v54, v46, s2\n"
    "v_perm_b32 v19, v62, v58, s2\n"
    "v_perm_b32 v28, v35, v43, s1\n"
    "v_perm_b32 v29, v51, v39, s1\n"
    "v_perm_b32 v30, v55, v47, s1\n"
    "v_perm_b32 v31, v63, v59, s1\n"
    "v_perm_b32 v20, v35, v43, s2\n"
    "v_perm_b32 v21, v51, v39, s2\n"
    "v_perm_b32 v22, v55, v47, s2\n"
    "v_perm_b32 v23, v63, v59, s2\n"
    "v_mov_b32_e32 v32, 0xfffff\n"
    ".Lfd_R_AS_5:\n"
    "ds_read_b64 v[38:39], v64 offset:24\n"
    "ds_read_b128 v[34:37], v64 offset:32\n"
    "v_subrev_co_u32_e32 v32, vcc, 1, v32\n"
    "s_waitcnt lgkmcnt(0)\n"
    "v_bitop3_b32 v33, v34, v35, v38 bitop3:0x80\n"
    "v_bitop3_b32 v33, v33, v37, v36 bitop3:0x80\n"
    "s_nop 0\n"
    "v_readfirstlane_b32 s1, v33\n"
    "s_cmp_lg_u32 s1, 0\n"
    "s_cselect_b64 s[8:9], -1, 0\n"
    "s_or_b64 s[8:9], s[8:9], vcc\n"
    "s_andn2_b64 vcc, exec, s[8:9]\n"
    "v_readfirstlane_b32 s1, v39\n"
    "s_cbranch_vccnz .Lfd_R_AS_5\n"
    ".Lfd_R_AS_6:\n"
    "s_memrealtime s[8:9]\n"
    "s_waitcnt lgkmcnt(0)\n"
    "s_sub_i32 s2, s8, s1\n"
    "s_addk_i32 s2, 0x80\n"
    "s_cmpk_lt_u32 s2, 0x13e\n"
    "s_cbranch_scc1 .Lfd_R_AS_6\n"
    "buffer_load_dwordx4 v[48:51], v68, s[4:7], 0 offen sc1\n"
    "buffer_load_dwordx4 v[44:47], v68, s[4:7], 0 offen offset:1024 sc1\n"
    "v_add_u32_e32 v54, 0x400, v68\n"
    "buffer_load_dwordx4 v[40:43], v68, s[4:7], 0 offen offset:2048 sc1\n"
    "v_add_u32_e32 v55, 0x800, v68\n"
    "buffer_load_dword v62, v69, s[4:7], 0 offen sc1\n"
    "buffer_load_dwordx4 v[36:39], v68, s[4:7], 0 offen offset:3072 sc1\n"
    "v_add_u32_e32 v56, 0xc00, v68\n"
    "v_mov_b32_e32 v52, 0xfffff\n"
    ".Lfd_R_AS_8:\n"
    "ds_read_b32 v53, v64 offset:4\n"
    "s_waitcnt lgkmcnt(1)\n"
    "ds_read_b128 v[32:35], v72\n"
    "v_subrev_co_u32_e32 v52, vcc, 1, v52\n"
    "s_waitcnt lgkmcnt(1)\n"
    "v_readfirstlane_b32 s1, v53\n"
    "s_cmp_lg_u32 s1, 0\n"
    "s_cselect_b64 s[8:9], -1, 0\n"
    "s_or_b64 s[8:9], s[8:9], vcc\n"
    "s_andn2_b64 vcc, exec, s[8:9]\n"
    "s_cbranch_vccnz .Lfd_R_AS_8\n"
    "s_lshl_b32 s0, s0, 5\n"
    "s_waitcnt lgkmcnt(0)\n"
    "v_lshlrev_b32_e32 v57, 16, v32\n"
    "v_lshlrev_b32_e32 v58, 16, v33\n"
    "v_lshlrev_b32_e32 v59, 16, v34\n"
    "v_lshlrev_b32_e32 v60, 16, v35\n"
    "s_sub_i32 s2, 2, s0\n"
    "v_mov_b32_e32 v61, 0x8000\n"
    "s_branch .Lfd_R_AS_11\n"
    ".Lfd_R_AS_10:\n"
    "s_andn2_b64 vcc, exec, s[0:1]\n"
    "s_cbranch_vccz .Lfd_R_AS_13\n"
    ".Lfd_R_AS_11:\n"
    "v_mov_b32_e32 v53, 0\n"
    "v_mov_b32_e32 v63, 0\n"
    "s_waitcnt vmcnt(4)\n"
    "v_add_u32_e32 v52, v48, v49\n"
    "v_dot2c_f32_bf16_e32 v53, v48, v32\n"
    "v_dot2c_f32_bf16_e32 v63, v48, v57\n"
    "v_mov_b32_e32 v48, 0\n"
    "v_mov_b32_e32 v72, 0\n"
    "v_dot2c_f32_bf16_e32 v48, v49, v32\n"
    "v_dot2c_f32_bf16_e32 v72, v49, v57\n"
    "v_mov_b32_e32 v49, 0\n"
    "v_mov_b32_e32 v73, 0\n"
    "v_add3_u32 v52, v52, v50, v51\n"
    "v_dot2c_f32_bf16_e32 v49, v50, v32\n"
    "v_dot2c_f32_bf16_e32 v73, v50, v57\n"
    "v_mov_b32_e32 v50, 0\n"
    "v_mov_b32_e32 v74, 0\n"
    "v_dot2c_f32_bf16_e32 v50, v51, v32\n"
    "v_dot2c_f32_bf16_e32 v74, v51, v57\n"
    "s_waitcnt vmcnt(3)\n"
    "v_add3_u32 v51, v52, v44, v45\n"
    "v_add3_u32 v51, v51, v46, v47\n"
    "v_dot2c_f32_bf16_e32 v53, v44, v33\n"
    "v_dot2c_f32_bf16_e32 v63, v44, v58\n"
    "v_dot2c_f32_bf16_e32 v48, v45, v33\n"
    "v_dot2c_f32_bf16_e32 v72, v45, v58\n"
    "v_dot2c_f32_bf16_e32 v49, v46, v33\n"
    "v_dot2c_f32_bf16_e32 v73, v46, v58\n"
    "v_dot2c_f32_bf16_e32 v50, v47, v33\n"
    "v_dot2c_f32_bf16_e32 v74, v47, v58\n"
    "s_waitcnt vmcnt(2)\n"
    "v_add3_u32 v44, v51, v40, v41\n"
    "v_add3_u32 v44, v44, v42, v43\n"
    "v_dot2c_f32_bf16_e32 v53, v40, v34\n"
    "v_dot2c_f32_bf16_e32 v63, v40, v59\n"
    "v_dot2c_f32_bf16_e32 v48, v41, v34\n"
    "v_dot2c_f32_bf16_e32 v72, v41, v59\n"
    "v_dot2c_f32_bf16_e32 v49, v42, v34\n"
    "v_dot2c_f32_bf16_e32 v73, v42, v59\n"
    "v_dot2c_f32_bf16_e32 v50, v43, v34\n"
    "v_dot2c_f32_bf16_e32 v74, v43, v59\n"
    "s_waitcnt vmcnt(1)\n"
    "v_sub_u32_e32 v40, v44, v62\n"
    "s_waitcnt vmcnt(0)\n"
    "v_add3_u32 v40, v40, v36, v37\n"
    "v_add3_u32 v40, v40, v38, v39\n"
    "v_dot2c_f32_bf16_e32 v53, v36, v35\n"
    "v_dot2c_f32_bf16_e32 v63, v36, v60\n"
    "v_add_u32_dpp v40, v40, v40 row_ror:8 row_mask:0xf bank_mask:0xf bound_ctrl:1\n"
    "v_dot2c_f32_bf16_e32 v48, v37, v35\n"
    "v_dot2c_f32_bf16_e32 v72, v37, v60\n"
    "v_add_u32_dpp v40, v40, v40 row_ror:4 row_mask:0xf bank_mask:0xf bound_ctrl:1\n"
    "v_dot2c_f32_bf16_e32 v49, v38, v35\n"
    "v_dot2c_f32_bf16_e32 v73, v38, v60\n"
    "v_add_u32_dpp v40, v40, v40 row_ror:2 row_mask:0xf bank_mask:0xf bound_ctrl:1\n"
    "v_dot2c_f32_bf16_e32 v50, v39, v35\n"
    "v_dot2c_f32_bf16_e32 v74, v39, v60\n"
    "v_add_u32_dpp v40, v40, v40 row_ror:1 row_mask:0xf bank_mask:0xf bound_ctrl:1\n"
    "s_nop 0\n"
    "v_readlane_b32 s0, v40, 0\n"
    "v_readlane_b32 s1, v40, 16\n"
    "s_add_i32 s0, s1, s0\n"
    "v_readlane_b32 s1, v40, 32\n"
    "v_permlane32_swap_b32_e32 v53, v49\n"
    "v_permlane32_swap_b32_e32 v63, v73\n"
    "s_add_i32 s0, s0, s1\n"
    "v_readlane_b32 s1, v40, 48\n"
    "v_add_f32_e32 v36, v53, v49\n"
    "v_add_f32_e32 v37, v63, v73\n"
    "v_permlane32_swap_b32_e32 v48, v50\n"
    "v_permlane32_swap_b32_e32 v72, v74\n"
    "s_add_i32 s0, s0, s1\n"
    "v_cvt_pk_bf16_f32 v52, v36, v37\n"
    "v_add_f32_e32 v36, v48, v50\n"
    "v_add_f32_e32 v37, v72, v74\n"
    "v_cvt_pk_bf16_f32 v53, v36, v37\n"
    "s_cmp_eq_u32 s0, s2\n"
    "s_mov_b64 s[0:1], -1\n"
    "s_cbranch_scc1 .Lfd_R_AS_10\n"
    "buffer_load_dwordx4 v[48:51], v68, s[4:7], 0 offen sc1\n"
    "buffer_load_dwordx4 v[44:47], v54, s[4:7], 0 offen sc1\n"
    "buffer_load_dwordx4 v[40:43], v55, s[4:7], 0 offen sc1\n"
    "buffer_load_dword v62, v69, s[4:7], 0 offen sc1\n"
    "buffer_load_dwordx4 v[36:39], v56, s[4:7], 0 offen sc1\n"
    "v_add_u32_e32 v61, -1, v61\n"
    "v_cmp_eq_u32_e64 s[0:1], 0, v61\n"
    "s_branch .Lfd_R_AS_10\n"
    ".Lfd_R_AS_13:\n"
    "s_waitcnt vmcnt(0)\n"
    "v_mov_b32_e32 v36, 0xfffff\n"
    "ds_write_b64 v71, v[52:53]\n"
    ".Lfd_R_AS_14:\n"
    "ds_read_b128 v[32:35], v70\n"
    "v_subrev_co_u32_e32 v36, vcc, 1, v36\n"
    "s_waitcnt lgkmcnt(0)\n"
    "v_max_u32_e32 v37, v32, v34\n"
    "v_cmp_ne_u32_e64 s[0:1], -1, v37\n"
    "s_cmp_eq_u64 s[0:1], exec\n"
    "s_cselect_b64 s[0:1], -1, 0\n"
    "s_or_b64 s[0:1], s[0:1], vcc\n"
    "s_andn2_b64 vcc, exec, s[0:1]\n"
    "s_cbranch_vccnz .Lfd_R_AS_14\n"
    "v_mfma_f32_16x16x32_bf16 v[4:7], v[32:35], v[4:7], 0\n"
    "v_mfma_f32_16x16x32_bf16 v[24:27], v[32:35], v[24:27], 0\n"
    "v_mfma_f32_16x16x32_bf16 v[12:15], v[32:35], v[12:15], 0\n"
    "v_mfma_f32_16x16x32_bf16 v[28:31], v[32:35], v[28:31], 0\n"
    "s_nop 4\n"
    "v_add_f32_e32 v4, v4, v5\n"
    "v_add_f32_e32 v5, v6, v7\n"
    "v_add_f32_e32 v36, v5, v4\n"
    "v_mfma_f32_16x16x32_bf16 v[0:3], v[32:35], v[0:3], 0\n"
    "v_add_f32_e32 v4, v24, v25\n"
    "v_add_f32_e32 v5, v26, v27\n"
    "v_add_f32_e32 v24, v5, v4\n"
    "v_mfma_f32_16x16x32_bf16 v[4:7], v[32:35], v[16:19], 0\n"
    "v_add_f32_e32 v12, v12, v13\n"
    "v_add_f32_e32 v13, v14, v15\n"
    "v_add_f32_e32 v16, v13, v12\n"
    "v_mfma_f32_16x16x32_bf16 v[8:11], v[32:35], v[8:11], 0\n"
    "v_add_f32_e32 v12, v28, v29\n"
    "v_add_f32_e32 v13, v30, v31\n"
    "v_add_f32_e32 v17, v13, v12\n"
    "v_mfma_f32_16x16x32_bf16 v[12:15], v[32:35], v[20:23], 0\n"
    "v_permlane32_swap_b32_e32 v36, v24\n"
    "v_permlane32_swap_b32_e32 v16, v17\n"
    "v_add_f32_e32 v18, v36, v24\n"
    "v_add_f32_e32 v16, v16, v17\n"
    "v_add_f32_e32 v0, v0, v1\n"
    "v_add_f32_e32 v1, v2, v3\n"
    "v_add_f32_e32 v1, v1, v0\n"
    "v_add_f32_e32 v0, v4, v5\n"
    "v_add_f32_e32 v2, v6, v7\n"
    "v_add_f32_e32 v2, v2, v0\n"
    "v_add_f32_e32 v0, v8, v9\n"
    "v_add_f32_e32 v3, v10, v11\n"
    "v_add_f32_e32 v3, v3, v0\n"
    "v_add_f32_e32 v0, v12, v13\n"
    "v_add_f32_e32 v4, v14, v15\n"
    "v_add_f32_e32 v4, v4, v0\n"
    "v_permlane32_swap_b32_e32 v1, v2\n"
    "s_nop 0\n"
    "v_permlane32_swap_b32_e32 v3, v4\n"
    "v_add_f32_e32 v1, v1, v2\n"
    "v_add_f32_e32 v2, v3, v4\n"
    "v_permlane16_swap_b32_e32 v18, v16\n"
    "s_nop 0\n"
    "v_permlane16_swap_b32_e32 v1, v2\n"
    "v_add_f32_e32 v0, v18, v16\n"
    "v_add_f32_e32 v1, v1, v2\n"
    "s_cmp_lg_u32 s3, 7\n"
    "ds_write_b64 v67, v[0:1]\n"
    "s_cbranch_scc1 .Lfd_R_AS_19\n"
    "v_mov_b32_e32 v8, 0xfffff\n"
    ".Lfd_R_AS_17:\n"
    "ds_read_b64 v[2:3], v66 offset:2048\n"
    "ds_read_b64 v[4:5], v66\n"
    "ds_read_b64 v[6:7], v66 offset:1024\n"
    "ds_read_b32 v9, v64 offset:56\n"
    "s_waitcnt lgkmcnt(1)\n"
    "v_max_u32_e32 v10, v4, v6\n"
    "s_waitcnt lgkmcnt(0)\n"
    "v_max3_u32 v10, v10, v2, v9\n"
    "v_cmp_ne_u32_e32 vcc, -1, v10\n"
    "s_cmp_eq_u64 vcc, exec\n"
    "s_cselect_b64 s[0:1], -1, 0\n"
    "v_subrev_co_u32_e32 v8, vcc, 1, v8\n"
    "s_or_b64 s[0:1], s[0:1], vcc\n"
    "s_andn2_b64 vcc, exec, s[0:1]\n"
    "s_cbranch_vccnz .Lfd_R_AS_17\n"
    "v_pk_add_f32 v[4:5], v[4:5], v[6:7]\n"
    "v_add_f32_e32 v0, v0, v2\n"
    "v_add_f32_e32 v1, v1, v3\n"
    "v_add_f32_e32 v0, v4, v0\n"
    "v_add_f32_e32 v1, v5, v1\n"
    "v_mul_f32_e32 v0, v0, v9\n"
    "v_mul_f32_e32 v1, v1, v9\n"
    "v_cvt_pk_bf16_f32 v0, v0, v1\n"
    "buffer_store_dword v0, v65, s[4:7], 0 offen sc1\n"
    ".Lfd_R_AS_19:\n"
    "s_endpgm\n"
    ".p2align 7\n"
    ".Lfd_R_AH:\n"
    "s_barrier\n"
    "s_load_dwordx2 s[4:5], s[0:1], 0x30\n"
    "s_barrier\n"
    "v_mov_b32_e32 v3, 0x27fd4\n"
    "ds_read_b32 v3, v3\n"
    "v_readfirstlane_b32 s8, v0\n"
    "s_lshr_b32 s3, s8, 6\n"
    "s_waitcnt lgkmcnt(0)\n"
    "s_and_b32 s5, s5, 0xffff\n"
    "s_add_i32 s9, s2, 0xffffff80\n"
    "v_lshrrev_b32_e32 v2, 4, v0\n"
    "v_readfirstlane_b32 s10, v3\n"
    "s_cmp_lg_u32 s3, 9\n"
    "s_cselect_b64 s[0:1], -1, 0\n"
    "s_lshl_b32 s11, s9, 8\n"
    "v_xor_b32_e32 v2, s10, v2\n"
    "s_and_b32 s11, s11, 0xffffc000\n"
    "v_lshlrev_b32_e32 v2, 13, v2\n"
    "v_and_b32_e32 v19, 63, v0\n"
    "s_add_i32 s11, s11, 0x68000\n"
    "v_and_b32_e32 v2, 0x2000, v2\n"
    "v_mov_b32_e32 v3, 0x27ff0\n"
    "v_cmp_lt_u32_e32 vcc, 31, v19\n"
    "v_or_b32_e32 v2, s11, v2\n"
    "s_lshl_b32 s11, s2, 7\n"
    "v_and_b32_e32 v1, 15, v0\n"
    "ds_read_b32 v3, v3\n"
    "s_and_b32 s11, s11, 0x1f80\n"
    "v_mov_b32_e32 v4, 0x7ff00000\n"
    "s_or_b64 vcc, vcc, s[0:1]\n"
    "v_or_b32_e32 v2, s11, v2\n"
    "s_waitcnt lgkmcnt(0)\n"
    "v_lshlrev_b32_e32 v3, 3, v1\n"
    "v_cndmask_b32_e32 v5, 0, v4, vcc\n"
    "v_or3_b32 v18, v2, v3, v5\n"
    "v_lshlrev_b32_e32 v2, 4, v0\n"
    "v_and_b32_e32 v22, 0x1f0, v2\n"
    "v_lshlrev_b32_e32 v2, 6, v0\n"
    "v_and_b32_e32 v2, 0x400, v2\n"
    "v_lshlrev_b32_e32 v3, 4, v1\n"
    "s_mov_b32 s11, 0x27800\n"
    "v_or3_b32 v21, v2, v3, s11\n"
    "v_lshlrev_b32_e32 v3, 3, v0\n"
    "v_cmp_lt_u32_e32 vcc, 7, v19\n"
    "v_lshlrev_b32_e32 v2, 11, v0\n"
    "v_and_b32_e32 v3, 24, v3\n"
    "s_lshr_b32 s9, s9, 4\n"
    "v_and_b32_e32 v2, 0x1e000, v2\n"
    "v_add_lshl_u32 v3, v3, s9, 8\n"
    "s_mov_b32 s9, 0x70000\n"
    "s_or_b64 vcc, vcc, s[0:1]\n"
    "v_add3_u32 v2, v2, v3, s9\n"
    "v_cndmask_b32_e32 v3, 0, v4, vcc\n"
    "s_mov_b32 s7, 0x20000\n"
    "s_mov_b32 s6, 0x600000\n"
    "v_or_b32_e32 v20, 0x27000, v22\n"
    "v_or_b32_e32 v23, v3, v2\n"
    "v_mov_b32_e32 v24, 1\n"
    "s_setprio 3\n"
    "s_lshl_b32 s0, s3, 2\n"
    "s_lshr_b32 s2, s2, 2\n"
    "s_add_i32 s0, s0, -16\n"
    "s_xor_b32 s2, s10, s2\n"
    "s_lshl_b32 s1, s0, 11\n"
    "s_lshl_b32 s2, s2, 13\n"
    "s_and_b32 s1, s1, 0xffffc000\n"
    "s_and_b32 s2, s2, 0x2000\n"
    "s_or_b32 s1, s1, s2\n"
    "s_lshl_b32 s2, s8, 6\n"
    "s_and_b32 s2, s2, 0x1000\n"
    "s_or_b32 s1, s1, s2\n"
    "s_lshl_b32 s0, s0, 10\n"
    "s_add_i32 s1, s1, 0x58000\n"
    "s_add_i32 s0, s0, 0x20000\n"
    "v_bitop3_b32 v2, s3, v19, 3 bitop3:0x6c\n"
    "v_lshl_or_b32 v25, v19, 4, s1\n"
    "v_lshl_or_b32 v26, v2, 4, s0\n"
    "s_barrier\n"
    "buffer_load_dwordx4 v[2:5], v25, s[4:7], 0 offen sc1\n"
    "buffer_load_dwordx4 v[6:9], v25, s[4:7], 0 offen offset:1024 sc1\n"
    "buffer_load_dwordx4 v[10:13], v25, s[4:7], 0 offen offset:2048 sc1\n"
    "buffer_load_dwordx4 v[14:17], v25, s[4:7], 0 offen offset:3072 sc1\n"
    "s_waitcnt vmcnt(3)\n"
    "v_max_u32_e32 v27, v2, v3\n"
    "v_max3_u32 v27, v27, v4, v5\n"
    "ds_write_b128 v26, v[2:5]\n"
    "s_waitcnt vmcnt(2)\n"
    "v_max3_u32 v2, v27, v6, v7\n"
    "v_xor_b32_e32 v27, 64, v26\n"
    "v_max3_u32 v2, v2, v8, v9\n"
    "ds_write_b128 v27, v[6:9] offset:1024\n"
    "s_waitcnt vmcnt(1)\n"
    "v_max3_u32 v2, v2, v10, v11\n"
    "v_xor_b32_e32 v28, 0x80, v26\n"
    "v_max3_u32 v2, v2, v12, v13\n"
    "ds_write_b128 v28, v[10:13] offset:2048\n"
    "s_waitcnt vmcnt(0)\n"
    "v_max3_u32 v2, v2, v14, v15\n"
    "v_xor_b32_e32 v29, 0xc0, v26\n"
    "v_max3_u32 v2, v2, v16, v17\n"
    "ds_write_b128 v29, v[14:17] offset:3072\n"
    "v_cmp_ne_u32_e32 vcc, -1, v2\n"
    "s_cmp_eq_u64 vcc, exec\n"
    "s_cbranch_scc1 .Lfd_R_AH_4\n"
    "v_add_u32_e32 v30, 0x400, v25\n"
    "v_add_u32_e32 v31, 0x800, v25\n"
    "v_add_u32_e32 v32, 0xc00, v25\n"
    "v_mov_b32_e32 v33, 0x7fff\n"
    ".Lfd_R_AH_2:\n"
    "buffer_load_dwordx4 v[2:5], v25, s[4:7], 0 offen sc1\n"
    "buffer_load_dwordx4 v[6:9], v30, s[4:7], 0 offen sc1\n"
    "buffer_load_dwordx4 v[10:13], v31, s[4:7], 0 offen sc1\n"
    "buffer_load_dwordx4 v[14:17], v32, s[4:7], 0 offen sc1\n"
    "v_subrev_co_u32_e32 v33, vcc, 1, v33\n"
    "s_waitcnt vmcnt(3)\n"
    "v_max_u32_e32 v34, v2, v3\n"
    "v_max3_u32 v34, v34, v4, v5\n"
    "s_waitcnt vmcnt(2)\n"
    "v_max3_u32 v34, v34, v6, v7\n"
    "v_max3_u32 v34, v34, v8, v9\n"
    "s_waitcnt vmcnt(1)\n"
    "v_max3_u32 v34, v34, v10, v11\n"
    "v_max3_u32 v34, v34, v12, v13\n"
    "s_waitcnt vmcnt(0)\n"
    "v_max3_u32 v34, v34, v14, v15\n"
    "v_max3_u32 v34, v34, v16, v17\n"
    "v_cmp_ne_u32_e64 s[0:1], -1, v34\n"
    "s_cmp_eq_u64 s[0:1], exec\n"
    "s_cselect_b64 s[0:1], -1, 0\n"
    "s_or_b64 s[0:1], s[0:1], vcc\n"
    "s_andn2_b64 vcc, exec, s[0:1]\n"
    "s_cbranch_vccnz .Lfd_R_AH_2\n"
    "ds_write_b128 v26, v[2:5]\n"
    "ds_write_b128 v27, v[6:9] offset:1024\n"
    "ds_write_b128 v28, v[10:13] offset:2048\n"
    "ds_write_b128 v29, v[14:17] offset:3072\n"
    ".Lfd_R_AH_4:\n"
    "s_and_b32 s2, s3, 3\n"
    "s_waitcnt lgkmcnt(0)\n"
    "s_barrier\n"
    "s_cmp_eq_u32 s3, 9\n"
    "s_cselect_b64 s[0:1], -1, 0\n"
    "s_cmp_lg_u32 s3, 9\n"
    "s_cbranch_scc1 .Lfd_R_AH_6\n"
    "s_memrealtime s[8:9]\n"
    "s_waitcnt lgkmcnt(0)\n"
    "s_not_b32 s3, s8\n"
    "v_mov_b32_e32 v2, s3\n"
    "buffer_atomic_umin v2, v23, s[4:7], 0 offen offset:4 sc1\n"
    "buffer_atomic_add v24, v23, s[4:7], 0 offen sc1\n"
    ".Lfd_R_AH_6:\n"
    "v_lshlrev_b32_e32 v0, 5, v0\n"
    "v_and_b32_e32 v0, 0x400, v0\n"
    "v_lshl_or_b32 v0, s2, 12, v0\n"
    "s_mov_b32 s3, 0x24000\n"
    "v_or3_b32 v0, v0, v22, s3\n"
    "s_barrier\n"
    "s_cmp_eq_u32 s2, 3\n"
    "s_mov_b64 s[2:3], -1\n"
    "s_cbranch_scc1 .Lfd_R_AH_8\n"
    "s_mov_b32 s8, -1\n"
    "s_mov_b32 s9, s8\n"
    "s_mov_b32 s10, s8\n"
    "s_mov_b32 s11, s8\n"
    "v_mov_b64_e32 v[2:3], s[8:9]\n"
    "v_mov_b64_e32 v[4:5], s[10:11]\n"
    "s_mov_b64 s[2:3], 0\n"
    "ds_write_b128 v0, v[2:5]\n"
    "ds_write_b128 v0, v[2:5] offset:2048\n"
    ".Lfd_R_AH_8:\n"
    "s_andn2_b64 vcc, exec, s[2:3]\n"
    "s_cbranch_vccnz .Lfd_R_AH_10\n"
    "v_mov_b32_e32 v0, 0x27000\n"
    "v_mov_b32_e32 v2, -1\n"
    "v_lshl_or_b32 v0, v19, 3, v0\n"
    "v_mov_b32_e32 v3, v2\n"
    "ds_write_b64 v0, v[2:3]\n"
    "v_cmp_lt_u32_e32 vcc, 13, v1\n"
    "v_mov_b32_e32 v2, 0x27400\n"
    "v_lshl_or_b32 v1, v1, 2, v2\n"
    "v_cndmask_b32_e64 v0, 0, -1, vcc\n"
    "ds_write_b32 v1, v0\n"
    ".Lfd_R_AH_10:\n"
    "s_setprio 3\n"
    "s_waitcnt lgkmcnt(0)\n"
    "s_barrier\n"
    "s_barrier\n"
    "s_andn2_b64 vcc, exec, s[0:1]\n"
    "s_cbranch_vccnz .Lfd_R_AH_14\n"
    "ds_read_b128 v[0:3], v21\n"
    "v_mov_b32_e32 v8, 0xfffff\n"
    ".Lfd_R_AH_12:\n"
    "s_sleep 1\n"
    "ds_read_b128 v[4:7], v20\n"
    "v_subrev_co_u32_e32 v8, vcc, 1, v8\n"
    "s_waitcnt lgkmcnt(0)\n"
    "v_max_u32_e32 v9, v4, v5\n"
    "v_max3_u32 v9, v9, v6, v7\n"
    "v_cmp_ne_u32_e64 s[0:1], -1, v9\n"
    "s_cmp_eq_u64 s[0:1], exec\n"
    "s_cselect_b64 s[0:1], -1, 0\n"
    "s_or_b64 s[0:1], s[0:1], vcc\n"
    "s_andn2_b64 vcc, exec, s[0:1]\n"
    "s_cbranch_vccnz .Lfd_R_AH_12\n"
    "v_maximum3_f32 v0, v0, v1, v1\n"
    "v_maximum3_f32 v0, v0, v2, v3\n"
    "v_add_f32_e32 v1, v4, v5\n"
    "v_add_f32_e32 v2, v6, v7\n"
    "v_add_f32_e32 v1, v1, v2\n"
    "buffer_store_dwordx2 v[0:1], v18, s[4:7], 0 offen sc1\n"
    ".Lfd_R_AH_14:\n"
    "s_endpgm\n"
    ".p2align 7\n"
    ".Lfd_R_O:\n"
    "s_waitcnt lgkmcnt(0)\n"
    "s_mov_b32 s4, s94\n"
    "s_memrealtime s[8:9]\n"
    "s_load_dwordx2 s[6:7], s[0:1], 0x40\n"
    "s_waitcnt lgkmcnt(0)\n"
    "s_sub_i32 s3, s8, s4\n"
    "s_cmpk_gt_u32 s3, 0x1c1\n"
    "v_readfirstlane_b32 s3, v0\n"
    "s_cbranch_scc1 .Lfd_R_O_2\n"
    ".Lfd_R_O_1:\n"
    "s_sleep 2\n"
    "s_memrealtime s[8:9]\n"
    "s_waitcnt lgkmcnt(0)\n"
    "s_sub_i32 s5, s8, s4\n"
    "s_cmpk_lt_u32 s5, 0x1c2\n"
    "s_cbranch_scc1 .Lfd_R_O_1\n"
    ".Lfd_R_O_2:\n"
    "s_load_dwordx2 s[4:5], s[0:1], 0x30\n"
    "s_lshr_b32 s0, s2, 3\n"
    "s_add_i32 s0, s0, -9\n"
    "s_lshl_b32 s9, s2, 7\n"
    "s_lshl_b32 s1, s0, 8\n"
    "s_and_b32 s9, s9, 0x80\n"
    "s_lshr_b32 s8, s3, 6\n"
    "s_waitcnt lgkmcnt(0)\n"
    "s_and_b32 s5, s5, 0xffff\n"
    "s_or_b32 s1, s1, s9\n"
    "s_cmp_lt_u32 s0, 4\n"
    "s_cselect_b32 s0, s1, 0x400\n"
    "s_lshl_b32 s1, s2, 9\n"
    "s_and_b32 s1, s1, 0xc00\n"
    "v_bfe_u32 v2, v0, 3, 3\n"
    "v_or_b32_e32 v2, s1, v2\n"
    "v_lshlrev_b32_e32 v4, 4, v0\n"
    "v_lshlrev_b32_e32 v5, 1, v0\n"
    "v_lshl_add_u32 v3, s8, 7, v2\n"
    "v_and_b32_e32 v2, 0x70, v4\n"
    "v_and_b32_e32 v5, 32, v5\n"
    "v_bitop3_b32 v2, s0, v2, v5 bitop3:0xf6\n"
    "s_movk_i32 s0, 0x480\n"
    "v_mad_u64_u32 v[2:3], s[0:1], v3, s0, v[2:3]\n"
    "v_xor_b32_e32 v3, 64, v2\n"
    "v_add_u32_e32 v6, 0x2000, v2\n"
    "v_add_u32_e32 v7, 0x4000, v2\n"
    "v_add_u32_e32 v5, 0x9000, v3\n"
    "s_lshl_b32 s0, s8, 14\n"
    "v_add_u32_e32 v8, 0x6000, v2\n"
    "s_add_u32 m0, s0, 0\n"
    "s_nop 0\n"
    "global_load_lds_dwordx4 v2, s[6:7] nt\n"
    "global_load_lds_dwordx4 v6, s[6:7] offset:1024 nt\n"
    "global_load_lds_dwordx4 v7, s[6:7] offset:2048 nt\n"
    "global_load_lds_dwordx4 v8, s[6:7] offset:3072 nt\n"
    "v_add_u32_e32 v6, 0xb000, v3\n"
    "v_add_u32_e32 v7, 0xd000, v3\n"
    "v_add_u32_e32 v8, 0xf000, v3\n"
    "s_add_u32 m0, s0, 0x1000\n"
    "s_nop 0\n"
    "global_load_lds_dwordx4 v5, s[6:7] nt\n"
    "global_load_lds_dwordx4 v6, s[6:7] offset:1024 nt\n"
    "global_load_lds_dwordx4 v7, s[6:7] offset:2048 nt\n"
    "global_load_lds_dwordx4 v8, s[6:7] offset:3072 nt\n"
    "v_add_u32_e32 v5, 0x12000, v2\n"
    "v_add_u32_e32 v6, 0x14000, v2\n"
    "v_add_u32_e32 v7, 0x16000, v2\n"
    "v_add_u32_e32 v2, 0x18000, v2\n"
    "s_add_u32 m0, s0, 0x2000\n"
    "s_nop 0\n"
    "global_load_lds_dwordx4 v5, s[6:7] nt\n"
    "global_load_lds_dwordx4 v6, s[6:7] offset:1024 nt\n"
    "global_load_lds_dwordx4 v7, s[6:7] offset:2048 nt\n"
    "global_load_lds_dwordx4 v2, s[6:7] offset:3072 nt\n"
    "v_add_u32_e32 v2, 0x1b000, v3\n"
    "v_add_u32_e32 v5, 0x1d000, v3\n"
    "v_add_u32_e32 v6, 0x1f000, v3\n"
    "v_add_u32_e32 v3, 0x21000, v3\n"
    "s_add_u32 m0, s0, 0x3000\n"
    "s_nop 0\n"
    "global_load_lds_dwordx4 v2, s[6:7] nt\n"
    "global_load_lds_dwordx4 v5, s[6:7] offset:1024 nt\n"
    "global_load_lds_dwordx4 v6, s[6:7] offset:2048 nt\n"
    "global_load_lds_dwordx4 v3, s[6:7] offset:3072 nt\n"
    "v_bfe_u32 v2, v0, 2, 4\n"
    "v_lshlrev_b32_e32 v5, 3, v0\n"
    "v_bfe_u32 v6, v0, 1, 1\n"
    "v_and_b32_e32 v7, 2, v2\n"
    "v_and_b32_e32 v2, 4, v2\n"
    "v_bfe_u32 v1, v0, 4, 2\n"
    "v_lshlrev_b32_e32 v3, 5, v0\n"
    "v_and_b32_e32 v5, 8, v5\n"
    "v_or3_b32 v2, v6, v7, v2\n"
    "s_movk_i32 s1, 0x180\n"
    "v_lshlrev_b32_e32 v2, 4, v2\n"
    "v_lshlrev_b32_e32 v6, 12, v1\n"
    "v_and_or_b32 v3, v3, s1, v5\n"
    "v_or3_b32 v2, v3, v2, v6\n"
    "s_movk_i32 s1, 0x60\n"
    "v_mov_b32_e32 v7, s0\n"
    "v_or_b32_e32 v3, s0, v2\n"
    "v_bitop3_b32 v5, v2, 32, s0 bitop3:0x36\n"
    "v_bitop3_b32 v6, v2, 64, s0 bitop3:0x36\n"
    "v_bitop3_b32 v2, v2, s1, v7 bitop3:0x36\n"
    "s_lshl_b32 s0, s2, 13\n"
    "s_lshl_b32 s1, s2, 11\n"
    "s_and_b32 s0, s0, 0x4000\n"
    "s_and_b32 s1, s1, 0x2000\n"
    "v_mov_b32_e32 v8, 0x27fd4\n"
    "s_or_b32 s0, s0, s1\n"
    "v_lshlrev_b32_e32 v7, 6, v0\n"
    "v_and_b32_e32 v4, 48, v4\n"
    "s_movk_i32 s1, 0x300\n"
    "ds_read_b32 v8, v8\n"
    "v_lshlrev_b32_e32 v1, 6, v1\n"
    "v_and_or_b32 v4, v7, s1, v4\n"
    "v_or3_b32 v1, v4, v1, s0\n"
    "v_lshl_add_u32 v1, s8, 10, v1\n"
    "v_add_u32_e32 v1, 0x78000, v1\n"
    "s_and_b32 s0, s3, 0x3fffffc0\n"
    "s_waitcnt lgkmcnt(0)\n"
    "v_xor_b32_e32 v69, v8, v1\n"
    "v_and_or_b32 v0, v0, 63, s0\n"
    "v_mov_b32_e32 v1, 0x26c00\n"
    "v_lshl_add_u32 v68, v0, 2, v1\n"
    "s_waitcnt vmcnt(0)\n"
    "ds_read_b64_tr_b16 v[24:25], v3\n"
    "ds_read_b64_tr_b16 v[26:27], v3 offset:512\n"
    "ds_read_b64_tr_b16 v[28:29], v3 offset:1024\n"
    "ds_read_b64_tr_b16 v[30:31], v3 offset:1536\n"
    "ds_read_b64_tr_b16 v[36:37], v3 offset:2048\n"
    "ds_read_b64_tr_b16 v[38:39], v3 offset:2560\n"
    "ds_read_b64_tr_b16 v[40:41], v3 offset:3072\n"
    "ds_read_b64_tr_b16 v[42:43], v3 offset:3584\n"
    "ds_read_b64_tr_b16 v[44:45], v5\n"
    "ds_read_b64_tr_b16 v[46:47], v5 offset:512\n"
    "ds_read_b64_tr_b16 v[48:49], v5 offset:1024\n"
    "ds_read_b64_tr_b16 v[50:51], v5 offset:1536\n"
    "ds_read_b64_tr_b16 v[52:53], v5 offset:2048\n"
    "ds_read_b64_tr_b16 v[54:55], v5 offset:2560\n"
    "ds_read_b64_tr_b16 v[56:57], v5 offset:3072\n"
    "ds_read_b64_tr_b16 v[58:59], v5 offset:3584\n"
    "ds_read_b64_tr_b16 v[60:61], v6\n"
    "ds_read_b64_tr_b16 v[62:63], v6 offset:512\n"
    "ds_read_b64_tr_b16 v[64:65], v6 offset:1024\n"
    "ds_read_b64_tr_b16 v[66:67], v6 offset:1536\n"
    "ds_read_b64_tr_b16 v[32:33], v6 offset:2048\n"
    "ds_read_b64_tr_b16 v[34:35], v6 offset:2560\n"
    "ds_read_b64_tr_b16 v[20:21], v6 offset:3072\n"
    "ds_read_b64_tr_b16 v[22:23], v6 offset:3584\n"
    "ds_read_b64_tr_b16 v[12:13], v2\n"
    "ds_read_b64_tr_b16 v[14:15], v2 offset:512\n"
    "ds_read_b64_tr_b16 v[8:9], v2 offset:1024\n"
    "ds_read_b64_tr_b16 v[10:11], v2 offset:1536\n"
    "ds_read_b64_tr_b16 v[4:5], v2 offset:2048\n"
    "ds_read_b64_tr_b16 v[6:7], v2 offset:2560\n"
    "ds_read_b64_tr_b16 v[0:1], v2 offset:3072\n"
    "ds_read_b64_tr_b16 v[2:3], v2 offset:3584\n"
    "s_mov_b32 s7, 0x20000\n"
    "s_mov_b32 s6, 0x600000\n"
    "v_mov_b32_e32 v70, 0x7fff\n"
    ".Lfd_R_O_3:\n"
    "buffer_load_dwordx4 v[16:19], v69, s[4:7], 0 offen sc1\n"
    "v_subrev_co_u32_e32 v70, vcc, 1, v70\n"
    "s_waitcnt vmcnt(0)\n"
    "v_max_u32_e32 v71, v16, v17\n"
    "v_max3_u32 v71, v71, v18, v19\n"
    "v_cmp_ne_u32_e64 s[0:1], -1, v71\n"
    "s_cmp_eq_u64 s[0:1], exec\n"
    "s_cselect_b64 s[0:1], -1, 0\n"
    "s_or_b64 s[0:1], s[0:1], vcc\n"
    "s_andn2_b64 vcc, exec, s[0:1]\n"
    "s_cbranch_vccnz .Lfd_R_O_3\n"
    "s_waitcnt lgkmcnt(14)\n"
    "v_mfma_f32_16x16x32_bf16 v[24:27], v[16:19], v[24:27], 0\n"
    "v_mfma_f32_16x16x32_bf16 v[26:29], v[16:19], v[28:31], 0\n"
    "v_mfma_f32_16x16x32_bf16 v[28:31], v[16:19], v[36:39], 0\n"
    "v_mfma_f32_16x16x32_bf16 v[36:39], v[16:19], v[40:43], 0\n"
    "v_mfma_f32_16x16x32_bf16 v[40:43], v[16:19], v[44:47], 0\n"
    "s_nop 5\n"
    "v_add_f32_e32 v28, v24, v27\n"
    "v_mfma_f32_16x16x32_bf16 v[42:45], v[16:19], v[48:51], 0\n"
    "v_mfma_f32_16x16x32_bf16 v[44:47], v[16:19], v[52:55], 0\n"
    "v_mfma_f32_16x16x32_bf16 v[52:55], v[16:19], v[60:63], 0\n"
    "v_mfma_f32_16x16x32_bf16 v[48:51], v[16:19], v[56:59], 0\n"
    "s_waitcnt lgkmcnt(12)\n"
    "v_mfma_f32_16x16x32_bf16 v[54:57], v[16:19], v[64:67], 0\n"
    "s_waitcnt lgkmcnt(10)\n"
    "v_mfma_f32_16x16x32_bf16 v[24:27], v[16:19], v[32:35], 0\n"
    "s_waitcnt lgkmcnt(8)\n"
    "v_mfma_f32_16x16x32_bf16 v[20:23], v[16:19], v[20:23], 0\n"
    "s_waitcnt lgkmcnt(6)\n"
    "v_mfma_f32_16x16x32_bf16 v[12:15], v[16:19], v[12:15], 0\n"
    "s_nop 3\n"
    "v_add_f32_e32 v24, v30, v39\n"
    "s_nop 0\n"
    "v_add_f32_e32 v20, v40, v43\n"
    "v_add_f32_e32 v21, v46, v51\n"
    "s_waitcnt lgkmcnt(4)\n"
    "v_mfma_f32_16x16x32_bf16 v[8:11], v[16:19], v[8:11], 0\n"
    "v_add_f32_e32 v14, v52, v55\n"
    "s_waitcnt lgkmcnt(2)\n"
    "v_mfma_f32_16x16x32_bf16 v[4:7], v[16:19], v[4:7], 0\n"
    "s_nop 4\n"
    "v_add_f32_e32 v8, v26, v23\n"
    "s_waitcnt lgkmcnt(0)\n"
    "v_mfma_f32_16x16x32_bf16 v[0:3], v[16:19], v[0:3], 0\n"
    "v_add_f32_e32 v5, v12, v9\n"
    "v_add_f32_e32 v24, v28, v24\n"
    "v_add_f32_e32 v13, v20, v21\n"
    "s_nop 4\n"
    "v_add_f32_e32 v0, v6, v3\n"
    "v_add_f32_e32 v4, v14, v8\n"
    "v_add_f32_e32 v0, v5, v0\n"
    "s_nop 0\n"
    "v_permlane32_swap_b32_e32 v24, v4\n"
    "v_permlane32_swap_b32_e32 v13, v0\n"
    "v_add_f32_e32 v1, v24, v4\n"
    "v_add_f32_e32 v0, v13, v0\n"
    "s_nop 1\n"
    "v_permlane16_swap_b32_e32 v1, v0\n"
    "v_add_f32_e32 v0, v1, v0\n"
    "ds_write_b32 v68, v0\n"
    "s_waitcnt lgkmcnt(0)\n"
    "s_barrier\n"
    "s_endpgm\n"
    ".p2align 7\n"
    ".Lfd_R_O8:\n"
    "s_load_dwordx2 s[4:5], s[0:1], 0x30\n"
    "s_lshr_b32 s0, s2, 3\n"
    "s_add_i32 s0, s0, -9\n"
    "s_lshl_b32 s3, s2, 6\n"
    "s_lshl_b32 s1, s0, 7\n"
    "s_and_b32 s3, s3, 64\n"
    "s_waitcnt lgkmcnt(0)\n"
    "s_and_b32 s5, s5, 0xffff\n"
    "s_or_b32 s1, s1, s3\n"
    "s_cmp_lt_u32 s0, 4\n"
    "s_cselect_b32 s0, s1, 0x200\n"
    "s_lshl_b32 s1, s2, 12\n"
    "s_and_b32 s1, s1, 0x4000\n"
    "s_bitcmp1_b32 s2, 1\n"
    "v_and_b32_e32 v0, 63, v0\n"
    "v_mov_b32_e32 v1, 0x26c00\n"
    "s_cselect_b32 s2, 0x900, 0\n"
    "v_lshl_or_b32 v6, v0, 2, v1\n"
    "s_or_b32 s1, s1, s2\n"
    "v_or_b32_e32 v0, s0, v0\n"
    "v_lshl_add_u32 v0, v0, 2, s1\n"
    "v_add_u32_e32 v8, 0x80000, v0\n"
    "s_mov_b32 s7, 0x20000\n"
    "v_add_u32_e32 v9, 0x2000, v8\n"
    "s_mov_b32 s6, 0x600000\n"
    "s_setprio 3\n"
    "s_waitcnt lgkmcnt(0)\n"
    "s_barrier\n"
    "ds_read2st64_b32 v[0:1], v6 offset1:1\n"
    "ds_read2st64_b32 v[2:3], v6 offset0:2 offset1:3\n"
    "ds_read2st64_b32 v[4:5], v6 offset0:4 offset1:5\n"
    "ds_read2st64_b32 v[6:7], v6 offset0:6 offset1:7\n"
    "s_waitcnt lgkmcnt(3)\n"
    "v_add_f32_e32 v0, v0, v1\n"
    "s_waitcnt lgkmcnt(2)\n"
    "v_add_f32_e32 v1, v2, v3\n"
    "v_add_f32_e32 v0, v0, v1\n"
    "s_waitcnt lgkmcnt(1)\n"
    "v_add_f32_e32 v1, v4, v5\n"
    "s_waitcnt lgkmcnt(0)\n"
    "v_add_f32_e32 v2, v6, v7\n"
    "v_add_f32_e32 v1, v1, v2\n"
    "v_add_f32_e32 v0, v0, v1\n"
    "buffer_store_dword v0, v8, s[4:7], 0 offen sc1\n"
    "buffer_store_dword v0, v9, s[4:7], 0 offen sc1\n"
    "s_endpgm\n"
    ".p2align 7\n"
    ".Lfd_R_R:\n"
    "s_load_dwordx2 s[4:5], s[0:1], 0x30\n"
    "s_load_dwordx2 s[6:7], s[0:1], 0x20\n"
    "s_lshr_b32 s3, s2, 3\n"
    "s_add_i32 s3, s3, -9\n"
    "s_lshl_b32 s9, s3, 1\n"
    "s_and_b32 s10, s2, 1\n"
    "s_waitcnt lgkmcnt(0)\n"
    "s_and_b32 s5, s5, 0xffff\n"
    "s_bfe_u32 s8, s2, 0x20001\n"
    "s_or_b32 s9, s9, s10\n"
    "s_cmp_lt_u32 s3, 4\n"
    "s_cselect_b32 s10, s9, 8\n"
    "s_mul_i32 s8, s8, 9\n"
    "s_add_i32 s3, s10, s8\n"
    "v_lshlrev_b32_e32 v2, 2, v0\n"
    "v_bfe_u32 v1, v0, 4, 2\n"
    "s_lshl_b32 s8, s3, 4\n"
    "v_and_b32_e32 v2, 12, v2\n"
    "v_or3_b32 v6, s8, v2, v1\n"
    "v_lshlrev_b32_e32 v2, 1, v6\n"
    "global_load_ushort v3, v2, s[6:7]\n"
    "v_mov_b32_e32 v5, 0\n"
    "s_waitcnt vmcnt(0)\n"
    "v_mov_b32 v4, v162\n"
    "v_mov_b32 v7, v163\n"
    "s_load_dwordx2 s[8:9], s[0:1], 0x60\n"
    "v_lshl_add_u64 v[20:21], v[4:5], 0, 1\n"
    "v_add_u32_e32 v21, v7, v21\n"
    "v_mov_b32_e32 v4, 0x27fd4\n"
    "ds_read_b32 v4, v4\n"
    "s_lshl_b32 s0, s2, 11\n"
    "s_and_b32 s0, s0, 0x2000\n"
    "s_waitcnt lgkmcnt(0)\n"
    "s_barrier\n"
    "v_and_b32_e32 v5, 0x7ffffffc, v2\n"
    "s_waitcnt lgkmcnt(0)\n"
    "v_readfirstlane_b32 s1, v4\n"
    "s_xor_b32 s11, s1, s0\n"
    "s_add_i32 s0, s11, 0x88000\n"
    "s_add_i32 s1, s11, 0x88d80\n"
    "s_add_i32 s12, s11, 0x80000\n"
    "s_add_i32 s13, s11, 0x80900\n"
    "s_add_i32 s14, s11, 0x84000\n"
    "s_add_i32 s15, s11, 0x84900\n"
    "v_lshlrev_b32_e32 v9, 2, v6\n"
    "v_and_b32_e32 v1, 63, v0\n"
    "s_mov_b32 s7, 0x20000\n"
    "s_mov_b32 s6, 0x600000\n"
    "v_add_u32_e32 v4, s0, v5\n"
    "v_add_u32_e32 v5, s1, v5\n"
    "v_add_u32_e32 v6, s12, v9\n"
    "v_add_u32_e32 v7, s13, v9\n"
    "v_add_u32_e32 v8, s14, v9\n"
    "v_add_u32_e32 v10, s15, v9\n"
    "v_mov_b32_e32 v12, 0x7fff\n"
    ".Lfd_R_R_1:\n"
    "s_sleep 8\n"
    "buffer_load_dword v16, v4, s[4:7], 0 offen sc1\n"
    "buffer_load_dword v14, v5, s[4:7], 0 offen sc1\n"
    "buffer_load_dword v9, v6, s[4:7], 0 offen sc1\n"
    "buffer_load_dword v11, v8, s[4:7], 0 offen sc1\n"
    "buffer_load_dword v13, v10, s[4:7], 0 offen sc1\n"
    "buffer_load_dword v15, v7, s[4:7], 0 offen sc1\n"
    "v_subrev_co_u32_e32 v12, vcc, 1, v12\n"
    "s_waitcnt vmcnt(3)\n"
    "v_max3_u32 v17, v16, v14, v9\n"
    "s_waitcnt vmcnt(1)\n"
    "v_max_u32_e32 v18, v11, v13\n"
    "s_waitcnt vmcnt(0)\n"
    "v_max3_u32 v17, v17, v15, v18\n"
    "v_cmp_ne_u32_e64 s[0:1], -1, v17\n"
    "s_cmp_eq_u64 s[0:1], exec\n"
    "s_cselect_b64 s[0:1], -1, 0\n"
    "s_or_b64 s[0:1], s[0:1], vcc\n"
    "s_andn2_b64 vcc, exec, s[0:1]\n"
    "s_cbranch_vccnz .Lfd_R_R_1\n"
    "v_lshlrev_b32_e32 v22, 16, v3\n"
    "v_and_b32_e32 v3, 16, v0\n"
    "v_and_b32_e32 v4, 0xffff0000, v16\n"
    "v_lshlrev_b32_e32 v5, 16, v16\n"
    "v_cmp_eq_u32_e32 vcc, 0, v3\n"
    "s_bitcmp1_b32 s2, 1\n"
    "s_cselect_b32 s0, 9, 0\n"
    "v_cndmask_b32_e32 v3, v4, v5, vcc\n"
    "v_and_b32_e32 v4, 0xffff0000, v14\n"
    "v_lshlrev_b32_e32 v5, 16, v14\n"
    "v_cndmask_b32_e32 v23, v4, v5, vcc\n"
    "v_add_f32_e32 v4, v15, v9\n"
    "v_add_f32_e32 v5, v13, v11\n"
    "v_add_f32_e32 v4, v5, v4\n"
    "s_add_i32 s10, s10, s0\n"
    "v_fmac_f32_e32 v22, v4, v3\n"
    "s_lshl_b32 s0, s10, 14\n"
    "v_mov_b32_e32 v4, 0x7ff00000\n"
    "v_cmp_lt_u32_e32 vcc, 31, v1\n"
    "v_and_b32_e32 v0, 12, v0\n"
    "s_add_i32 s11, s11, s0\n"
    "v_cndmask_b32_e32 v5, 0, v4, vcc\n"
    "v_cmp_eq_u32_e32 vcc, 0, v0\n"
    "v_lshl_add_u32 v3, v1, 4, s11\n"
    "v_add_u32_e32 v26, 0x10000, v3\n"
    "v_cndmask_b32_e64 v0, v4, 0, vcc\n"
    "v_or_b32_e32 v24, v2, v0\n"
    "v_or_b32_e32 v0, s3, v1\n"
    "v_cmp_eq_u32_e32 vcc, 0, v0\n"
    "v_add_u32_e32 v3, 0x1000, v26\n"
    "v_or_b32_e32 v27, v3, v5\n"
    "s_mov_b32 s11, 0x20000\n"
    "s_movk_i32 s10, 0x480\n"
    "s_and_b32 s9, s9, 0xffff\n"
    "v_cndmask_b32_e64 v25, v4, 0, vcc\n"
    "v_add_u32_e32 v28, 0x400, v26\n"
    "v_add_u32_e32 v29, 0x800, v26\n"
    "v_add_u32_e32 v30, 0xc00, v26\n"
    "v_mov_b32_e32 v31, 0x7fff\n"
    "s_sleep 16\n"
    ".Lfd_R_R_3:\n"
    "buffer_load_dwordx4 v[12:15], v26, s[4:7], 0 offen sc1\n"
    "buffer_load_dwordx4 v[16:19], v28, s[4:7], 0 offen sc1\n"
    "buffer_load_dwordx4 v[4:7], v29, s[4:7], 0 offen sc1\n"
    "buffer_load_dwordx4 v[8:11], v30, s[4:7], 0 offen sc1\n"
    "buffer_load_dwordx4 v[0:3], v27, s[4:7], 0 offen sc1\n"
    "v_subrev_co_u32_e32 v31, vcc, 1, v31\n"
    "s_waitcnt vmcnt(4)\n"
    "v_max_u32_e32 v32, v12, v13\n"
    "v_max3_u32 v32, v32, v14, v15\n"
    "s_waitcnt vmcnt(3)\n"
    "v_max3_u32 v32, v32, v16, v17\n"
    "v_max3_u32 v32, v32, v18, v19\n"
    "s_waitcnt vmcnt(2)\n"
    "v_max3_u32 v32, v32, v4, v5\n"
    "v_max3_u32 v32, v32, v6, v7\n"
    "s_waitcnt vmcnt(1)\n"
    "v_max3_u32 v32, v32, v8, v9\n"
    "v_max3_u32 v32, v32, v10, v11\n"
    "s_waitcnt vmcnt(0)\n"
    "v_max3_u32 v32, v32, v0, v1\n"
    "v_max3_u32 v32, v32, v2, v3\n"
    "v_cmp_ne_u32_e64 s[0:1], -1, v32\n"
    "s_cmp_eq_u64 s[0:1], exec\n"
    "s_cselect_b64 s[0:1], -1, 0\n"
    "s_or_b64 s[0:1], s[0:1], vcc\n"
    "s_andn2_b64 vcc, exec, s[0:1]\n"
    "s_cbranch_vccnz .Lfd_R_R_3\n"
    "v_pk_add_f32 v[14:15], v[14:15], v[18:19]\n"
    "v_pk_add_f32 v[12:13], v[12:13], v[16:17]\n"
    "v_pk_add_f32 v[6:7], v[6:7], v[10:11]\n"
    "v_pk_add_f32 v[4:5], v[4:5], v[8:9]\n"
    "v_pk_add_f32 v[6:7], v[14:15], v[6:7]\n"
    "v_pk_add_f32 v[4:5], v[12:13], v[4:5]\n"
    "v_pk_add_f32 v[2:3], v[6:7], v[2:3]\n"
    "v_pk_add_f32 v[0:1], v[4:5], v[0:1]\n"
    "s_nop 1\n"
    "v_permlane32_swap_b32_e32 v0, v2\n"
    "v_permlane32_swap_b32_e32 v1, v3\n"
    "v_add_f32_e32 v0, v0, v2\n"
    "v_add_f32_e32 v1, v1, v3\n"
    "s_nop 1\n"
    "v_permlane16_swap_b32_e32 v0, v1\n"
    "v_add_f32_e32 v0, v0, v1\n"
    "s_nop 1\n"
    "v_add_f32_dpp v0, v0, v0 row_ror:8 row_mask:0xf bank_mask:0xf bound_ctrl:1\n"
    "s_nop 1\n"
    "v_add_f32_dpp v0, v0, v0 row_ror:4 row_mask:0xf bank_mask:0xf bound_ctrl:1\n"
    "v_fmac_f32_e32 v22, v23, v0\n"
    "v_cvt_pk_bf16_f32 v0, v22, s0\n"
    "buffer_store_short v0, v24, s[8:11], 0 offen\n"
    "buffer_store_dwordx2 v[20:21], v25, s[4:7], 0 offen sc1\n"
    "s_endpgm\n"
    ".p2align 7\n"
    ".Lfd_R_M:\n"
    "v_readfirstlane_b32 s5, v0\n"
    "s_lshr_b32 s8, s2, 3\n"
    "s_lshr_b32 s4, s5, 6\n"
    "s_and_b32 s3, s2, 7\n"
    "s_add_i32 s8, s8, -9\n"
    "s_bfe_u32 s2, s2, 0x20001\n"
    "s_cmp_eq_u32 s8, 5\n"
    "s_cselect_b32 s9, 4, 12\n"
    "s_add_i32 s9, s9, s3\n"
    "s_cmp_eq_u32 s8, 4\n"
    "s_cselect_b32 s8, s2, s9\n"
    "s_cmpk_gt_u32 s5, 0x17f\n"
    "s_cselect_b64 s[2:3], -1, 0\n"
    "s_load_dwordx2 s[6:7], s[0:1], 0x28\n"
    "v_cndmask_b32_e64 v5, 0, 1, s[2:3]\n"
    "s_and_b64 s[2:3], s[2:3], exec\n"
    "v_and_b32_e32 v1, 63, v0\n"
    "s_cselect_b32 s2, -6, 0\n"
    "s_add_i32 s9, s2, s4\n"
    "v_min_u32_e32 v4, 47, v1\n"
    "v_mad_u64_u32 v[2:3], s[2:3], s9, 48, v[4:5]\n"
    "v_mov_b32_e32 v3, 0\n"
    "s_load_dwordx2 s[2:3], s[0:1], 0x8\n"
    "s_waitcnt lgkmcnt(0)\n"
    "v_lshl_add_u64 v[2:3], v[2:3], 2, s[6:7]\n"
    "global_load_dword v3, v[2:3], off\n"
    "v_bfe_u32 v7, v0, 3, 3\n"
    "v_lshlrev_b32_e32 v8, 4, v0\n"
    "v_lshlrev_b32_e32 v2, 1, v0\n"
    "v_bfe_u32 v9, v0, 2, 4\n"
    "v_mul_u32_u24_e32 v7, 0x1b00, v7\n"
    "v_and_b32_e32 v8, 0x70, v8\n"
    "v_and_b32_e32 v14, 32, v2\n"
    "v_bfe_u32 v13, v0, 1, 1\n"
    "v_and_b32_e32 v15, 2, v9\n"
    "v_and_b32_e32 v9, 4, v9\n"
    "v_bitop3_b32 v7, v8, v7, v14 bitop3:0xde\n"
    "v_or3_b32 v8, v13, v15, v9\n"
    "s_mul_i32 s6, s4, 0x280\n"
    "v_readfirstlane_b32 s10, v5\n"
    "s_add_i32 s11, s6, 0x24e00\n"
    "s_lshl_b32 s6, s8, 1\n"
    "s_or_b32 s8, s6, s10\n"
    "v_lshlrev_b32_e32 v10, 6, v0\n"
    "v_lshlrev_b32_e32 v11, 5, v0\n"
    "v_lshlrev_b32_e32 v12, 3, v0\n"
    "s_mul_i32 s9, s9, 0xa2000\n"
    "s_lshl_b32 s8, s8, 7\n"
    "v_and_b32_e32 v10, 0xc00, v10\n"
    "v_and_b32_e32 v11, 0x180, v11\n"
    "v_and_b32_e32 v12, 8, v12\n"
    "s_or_b32 s8, s8, s9\n"
    "v_or3_b32 v9, v10, v12, v11\n"
    "s_addk_i32 s8, 0x900\n"
    "v_mov_b32_e32 v6, 0x60\n"
    "s_mul_i32 s7, s4, 0x3000\n"
    "v_lshl_or_b32 v8, v8, 4, v9\n"
    "v_lshl_add_u32 v4, v4, 2, s11\n"
    "v_add_u32_e32 v5, s8, v7\n"
    "v_bitop3_b32 v47, s7, v6, v8 bitop3:0x36\n"
    "v_xor_b32_e32 v6, 64, v5\n"
    "v_add_u32_e32 v7, 0x1a800, v5\n"
    "v_add_u32_e32 v12, 0xd400, v6\n"
    "v_add_u32_e32 v13, 0x27c00, v6\n"
    "v_or_b32_e32 v44, s7, v8\n"
    "v_bitop3_b32 v45, s7, 32, v8 bitop3:0x36\n"
    "v_bitop3_b32 v46, s7, 64, v8 bitop3:0x36\n"
    "v_add_u32_e32 v8, 0x36000, v5\n"
    "v_add_u32_e32 v9, 0x50800, v5\n"
    "v_add_u32_e32 v17, 0x43400, v6\n"
    "v_add_u32_e32 v18, 0x5dc00, v6\n"
    "v_and_or_b32 v48, v0, 48, s11\n"
    "v_add_u32_e32 v10, 0x6c000, v5\n"
    "v_add_u32_e32 v11, 0x86800, v5\n"
    "v_add_u32_e32 v19, 0x79400, v6\n"
    "v_add_u32_e32 v6, 0x93c00, v6\n"
    "s_mov_b32 vcc_lo, 0xffff0000\n"
    "s_mov_b32 vcc_hi, vcc_lo\n"
    "s_waitcnt vmcnt(0)\n"
    "v_lshlrev_b32_e32 v14, 16, v3\n"
    "v_and_b32_e32 v3, 0xffff0000, v3\n"
    "v_mul_f32_e32 v15, 0xbfb8aa3b, v14\n"
    "v_mul_f32_e32 v16, 0xbfb8aa3b, v3\n"
    "v_exp_f32_e32 v15, v15\n"
    "v_exp_f32_e32 v16, v16\n"
    "v_add_f32_e32 v15, 1.0, v15\n"
    "v_add_f32_e32 v16, 1.0, v16\n"
    "v_rcp_f32_e32 v15, v15\n"
    "v_rcp_f32_e32 v16, v16\n"
    "v_mul_f32_e32 v14, v15, v14\n"
    "v_mul_f32_e32 v3, v16, v3\n"
    "v_cvt_pk_bf16_f32 v3, v14, v3\n"
    "ds_write_b32 v4, v3\n"
    "s_add_u32 m0, s7, 0\n"
    "s_nop 0\n"
    "global_load_lds_dwordx4 v5, s[2:3] nt\n"
    "global_load_lds_dwordx4 v12, s[2:3] offset:1024 nt\n"
    "global_load_lds_dwordx4 v7, s[2:3] offset:2048 nt\n"
    "global_load_lds_dwordx4 v13, s[2:3] offset:3072 nt\n"
    "s_nop 0\n"
    "s_add_u32 m0, s7, 0x1000\n"
    "s_nop 0\n"
    "global_load_lds_dwordx4 v8, s[2:3] nt\n"
    "global_load_lds_dwordx4 v17, s[2:3] offset:1024 nt\n"
    "global_load_lds_dwordx4 v9, s[2:3] offset:2048 nt\n"
    "global_load_lds_dwordx4 v18, s[2:3] offset:3072 nt\n"
    "s_nop 0\n"
    "s_add_u32 m0, s7, 0x2000\n"
    "s_nop 0\n"
    "global_load_lds_dwordx4 v10, s[2:3] nt\n"
    "global_load_lds_dwordx4 v19, s[2:3] offset:1024 nt\n"
    "global_load_lds_dwordx4 v11, s[2:3] offset:2048 nt\n"
    "global_load_lds_dwordx4 v6, s[2:3] offset:3072 nt\n"
    "s_waitcnt vmcnt(0)\n"
    "ds_read_b128 v[4:7], v48\n"
    "ds_read_b64_tr_b16 v[8:9], v44\n"
    "ds_read_b64_tr_b16 v[10:11], v44 offset:512\n"
    "ds_read_b64_tr_b16 v[12:13], v44 offset:4096\n"
    "ds_read_b64_tr_b16 v[14:15], v44 offset:4608\n"
    "ds_read_b128 v[16:19], v48 offset:64\n"
    "ds_read_b64_tr_b16 v[20:21], v45\n"
    "ds_read_b64_tr_b16 v[22:23], v45 offset:512\n"
    "ds_read_b64_tr_b16 v[24:25], v45 offset:4096\n"
    "ds_read_b64_tr_b16 v[26:27], v45 offset:4608\n"
    "s_waitcnt lgkmcnt(7)\n"
    "v_mfma_f32_16x16x32_bf16 v[8:11], v[4:7], v[8:11], 0\n"
    "ds_read_b64_tr_b16 v[28:29], v46\n"
    "ds_read_b64_tr_b16 v[30:31], v46 offset:512\n"
    "ds_read_b64_tr_b16 v[32:33], v46 offset:4096\n"
    "ds_read_b64_tr_b16 v[34:35], v46 offset:4608\n"
    "ds_read_b64_tr_b16 v[36:37], v47\n"
    "ds_read_b64_tr_b16 v[38:39], v47 offset:512\n"
    "ds_read_b64_tr_b16 v[40:41], v47 offset:4096\n"
    "ds_read_b64_tr_b16 v[42:43], v47 offset:4608\n"
    "s_mov_b32 s2, 0\n"
    "s_waitcnt lgkmcnt(10)\n"
    "v_mfma_f32_16x16x32_bf16 v[20:23], v[4:7], v[20:23], 0\n"
    "s_mov_b32 s3, -1\n"
    "s_waitcnt lgkmcnt(6)\n"
    "v_mfma_f32_16x16x32_bf16 v[28:31], v[4:7], v[28:31], 0\n"
    "s_waitcnt lgkmcnt(2)\n"
    "v_mfma_f32_16x16x32_bf16 v[4:7], v[4:7], v[36:39], 0\n"
    "v_mfma_f32_16x16x32_bf16 v[8:11], v[16:19], v[12:15], v[8:11]\n"
    "v_mfma_f32_16x16x32_bf16 v[12:15], v[16:19], v[24:27], v[20:23]\n"
    "ds_read_b128 v[24:27], v48 offset:128\n"
    "v_mfma_f32_16x16x32_bf16 v[20:23], v[16:19], v[32:35], v[28:31]\n"
    "s_waitcnt lgkmcnt(1)\n"
    "v_mfma_f32_16x16x32_bf16 v[4:7], v[16:19], v[40:43], v[4:7]\n"
    "ds_read_b64_tr_b16 v[16:17], v44 offset:8192\n"
    "ds_read_b64_tr_b16 v[18:19], v44 offset:8704\n"
    "s_waitcnt lgkmcnt(0)\n"
    "v_mfma_f32_16x16x32_bf16 v[8:11], v[24:27], v[16:19], v[8:11]\n"
    "ds_read_b64_tr_b16 v[16:17], v45 offset:8192\n"
    "ds_read_b64_tr_b16 v[18:19], v45 offset:8704\n"
    "s_waitcnt lgkmcnt(0)\n"
    "v_mfma_f32_16x16x32_bf16 v[10:13], v[24:27], v[16:19], v[12:15]\n"
    "s_nop 7\n"
    "ds_read_b64_tr_b16 v[12:13], v46 offset:8192\n"
    "ds_read_b64_tr_b16 v[14:15], v46 offset:8704\n"
    "s_waitcnt lgkmcnt(0)\n"
    "v_mfma_f32_16x16x32_bf16 v[12:15], v[24:27], v[12:15], v[20:23]\n"
    "s_nop 7\n"
    "ds_read_b64_tr_b16 v[14:15], v47 offset:8192\n"
    "ds_read_b64_tr_b16 v[16:17], v47 offset:8704\n"
    "s_waitcnt lgkmcnt(0)\n"
    "v_mfma_f32_16x16x32_bf16 v[4:7], v[24:27], v[14:17], v[4:7]\n"
    "v_cndmask_b32_e32 v3, v8, v10, vcc\n"
    "s_nop 6\n"
    "v_cndmask_b32_e32 v4, v12, v4, vcc\n"
    "v_cndmask_b32_e64 v3, v3, v4, s[2:3]\n"
    "s_and_b32 s2, s5, 0x3fffffc0\n"
    "v_or_b32_e32 v4, s2, v1\n"
    "v_mov_b32_e32 v5, 0x26c00\n"
    "v_lshl_add_u32 v4, v4, 2, v5\n"
    "ds_write_b32 v4, v3\n"
    "s_waitcnt lgkmcnt(0)\n"
    "s_barrier\n"
    "s_cmpk_gt_u32 s5, 0x7f\n"
    "s_cbranch_scc1 .Lfd_R_M_2\n"
    "s_mul_i32 s2, s4, 0x600\n"
    "v_lshl_or_b32 v1, v1, 2, s2\n"
    "v_add_u32_e32 v1, 0x26c00, v1\n"
    "ds_read2st64_b32 v[4:5], v1 offset1:1\n"
    "ds_read2st64_b32 v[6:7], v1 offset0:2 offset1:3\n"
    "ds_read2st64_b32 v[8:9], v1 offset0:4 offset1:5\n"
    "s_load_dwordx2 s[0:1], s[0:1], 0x30\n"
    "v_and_b32_e32 v0, 1, v0\n"
    "s_waitcnt lgkmcnt(0)\n"
    "v_add_f32_e32 v1, 0, v4\n"
    "v_add_f32_e32 v1, v1, v5\n"
    "v_add_f32_e32 v1, v1, v6\n"
    "v_add_f32_e32 v1, v1, v7\n"
    "v_add_f32_e32 v1, v1, v8\n"
    "v_add_f32_e32 v1, v1, v9\n"
    "s_or_b32 s4, s4, s6\n"
    "v_lshlrev_b32_e32 v4, 13, v0\n"
    "v_mov_b32_dpp v3, v1 quad_perm:[1,0,3,2] row_mask:0xf bank_mask:0xf bound_ctrl:1\n"
    "s_lshl_b32 s4, s4, 7\n"
    "v_and_b32_e32 v2, 0x7c, v2\n"
    "v_cmp_eq_u32_e32 vcc, 0, v0\n"
    "v_or3_b32 v2, s4, v4, v2\n"
    "s_and_b32 s1, s1, 0xffff\n"
    "v_cndmask_b32_e32 v0, v1, v3, vcc\n"
    "v_cndmask_b32_e32 v1, v3, v1, vcc\n"
    "s_mov_b32 s3, 0x20000\n"
    "s_mov_b32 s2, 0x600000\n"
    "v_add_u32_e32 v2, 0x88000, v2\n"
    "v_cvt_pk_bf16_f32 v0, v1, v0\n"
    "buffer_store_dword v0, v2, s[0:3], 0 offen sc1\n"
    ".Lfd_R_M_2:\n"
    "s_endpgm\n"
    ".Lfd_end:\n"
    :
    : "{s[0:1]}"(__builtin_amdgcn_kernarg_segment_ptr()), "{s2}"(__builtin_amdgcn_workgroup_id_x()), "{v0}"(__builtin_amdgcn_workitem_id_x())
    : "v1", "v2", "v3", "v4", "v5", "v6", "v7", "v8", "v9", "v10", "v11", "v12", "v13", "v14", "v15", "v16", "v17", "v18", "v19", "v20", "v21", "v22", "v23", "v24", "v25", "v26", "v27", "v28", "v29", "v30", "v31", "v32", "v33", "v34", "v35", "v36", "v37", "v38", "v39", "v40", "v41", "v42", "v43", "v44", "v45", "v46", "v47", "v48", "v49", "v50", "v51", "v52", "v53", "v54", "v55", "v56", "v57", "v58", "v59", "v60", "v61", "v62", "v63", "v64", "v65", "v66", "v67", "v68", "v69", "v70", "v71", "v72", "v73", "v74", "v75", "v76", "v77", "v78", "v79", "v80", "v81", "v82", "v83", "v84", "v85", "v86", "v87", "v88", "v89", "v90", "v91", "v92", "v93", "v94", "v95", "v96", "v97", "v98", "v99", "v100", "v101", "v102", "v103", "v104", "v105", "v106", "v107", "v108", "v109", "v110", "v111", "v112", "v113", "v114", "v115", "v116", "v117", "v118", "v119", "v120", "v121", "v122", "v123", "v124", "v125", "v126", "v127", "v128", "v129", "v130", "v131", "v132", "v133", "v134", "v135", "v136", "v137", "v138", "v139", "v140", "v141", "v142", "v143", "v144", "v145", "v146", "v147", "v148", "v149", "v150", "v151", "v152", "v153", "v154", "v155", "v156", "v157", "v158", "v159", "v160", "v161", "v162", "v163", "v164", "v165", "v166", "v167", "s3", "s4", "s5", "s6", "s7", "s8", "s9", "s10", "s11", "s12", "s13", "s14", "s15", "s16", "s17", "s18", "s19", "s20", "s21", "s22", "s23", "s24", "s25", "s26", "s27", "s28", "s29", "s30", "s31", "s33", "s34", "s35", "s36", "s37", "s38", "s39", "s40", "s41", "s42", "s43", "s44", "s45", "s46", "s47", "s48", "s49", "s50", "s51", "s52", "s53", "s54", "s55", "s56", "s57", "s58", "s59", "s60", "s61", "s62", "s63", "s64", "s65", "s66", "s67", "s68", "s69", "s70", "s71", "s72", "s73", "s74", "s75", "s76", "s77", "s78", "s79", "s80", "s81", "s82", "s83", "s84", "s85", "s86", "s87", "s88", "s89", "s90", "s91", "s92", "s93", "s94", "s95", "s96", "s97", "s98", "s99", "vcc", "scc", "memory");
  asm volatile("" ::"v"((__attribute__((address_space(3))) char*)smem));
#endif
}

extern "C" void kernel_launch(void* const* d_in, const int* in_sizes, int n_in, void* d_out, int out_size, void* d_ws, size_t ws_size,
                              hipStream_t stream) {
  void* ws = (void*)(((uintptr_t)d_ws + 2097151) & ~(uintptr_t)2097151);
  fused_decode<<<256, 768, 0, stream>>>(d_in[1], d_in[11], d_in[2], d_in[3], d_in[0], d_in[10], ws, d_in[4], d_in[5], d_in[7], d_in[8],
                                        d_in[9], d_out);
}
```
